```python
import torch

def _softmax(x, axis=-1):
    return torch.softmax(x, dim=axis)
def _gelu(x):
    return torch.nn.functional.gelu(x, approximate="tanh")
def _silu(x):
    return torch.nn.functional.silu(x)
def _sigmoid(x):
    return torch.sigmoid(x)
def _layernorm(x, axis=-1, eps=1e-5):
    m = x.mean(dim=axis, keepdim=True)
    v = x.var(dim=axis, keepdim=True, unbiased=False)
    return (x - m) / torch.sqrt(v + eps)
def _scatter_add(tgt, idx, src, axis=0):
    return tgt.clone().index_add_(axis, idx, src)
def _segment_sum(src, seg, n):
    out = torch.zeros((n,) + src.shape[1:], dtype=src.dtype)
    return out.index_add_(0, seg, src)
def _pad(x, pw):
    flat = tuple(v for pair in reversed(pw) for v in pair)
    return torch.nn.functional.pad(x, flat)

kernel_name = 'concat_heads__ssm_conv_h3__grouped_kv_mqa__prompt'

def setup_inputs():

    _w = (lambda a: (lambda t: t.to(torch.bfloat16) if t.is_floating_point() else t)(torch.as_tensor(a)))
    def _A_setup():
        B, S, D = 1, 2048, 640
        ks = 4
        return {
            "q": _w((lambda *s: torch.randn(*s, dtype=torch.bfloat16) * 0.05)(B, S, D)),
            "k": _w((lambda *s: torch.randn(*s, dtype=torch.bfloat16) * 0.4)(B, S, D)),
            "v": _w((lambda *s: torch.randn(*s, dtype=torch.bfloat16) * 0.2)(B, S, D)),
            "shift_k": _w((lambda *s: torch.randn(*s, dtype=torch.bfloat16) * 0.4)(D, ks) * 0.5),
            "ssm_k": _w((lambda *s: torch.randn(*s, dtype=torch.bfloat16) * 0.4)(D, S) * (1.0 / S ** 0.5)),
            "ssm_D": _w((lambda *s: torch.randn(*s, dtype=torch.bfloat16) * 0.2)(D)),
        }
    _A_in = _A_setup()

    def _B_setup():
        B, S, D, H = 1, 2048, 640, 32
        Dh = D // H
        return {
            "x": _w((lambda *s: torch.randn(*s, dtype=torch.bfloat16) * 0.05)(B, S, D)),
            "wq": _w((lambda *s: torch.randn(*s, dtype=torch.bfloat16) * 0.8)(D, H * Dh)),
            "wk": _w((lambda *s: torch.randn(*s, dtype=torch.bfloat16) * 0.8)(D, Dh)),
            "wv": _w((lambda *s: torch.randn(*s, dtype=torch.bfloat16) * 0.05)(D, Dh)),
            "wo": _w((lambda *s: torch.randn(*s, dtype=torch.bfloat16) * 0.05)(H * Dh, D)),
        }
    _B_in = _B_setup()

    return {
        "A_q": _A_in["q"],
        "A_k": _A_in["k"],
        "A_v": _A_in["v"],
        "A_shift_k": _A_in["shift_k"],
        "A_ssm_k": _A_in["ssm_k"],
        "A_ssm_D": _A_in["ssm_D"],
        "B_x": _B_in["x"],
        "B_wq": _B_in["wq"],
        "B_wk": _B_in["wk"],
        "B_wv": _B_in["wv"],
        "B_wo": _B_in["wo"],
    }

def reference(A_q, A_k, A_v, A_shift_k, A_ssm_k, A_ssm_D, B_x, B_wq, B_wk, B_wv, B_wo):
    def _A_ref(q, k, v, shift_k, ssm_k, ssm_D):
        B, S, D = q.shape
        ks = shift_k.shape[-1]
        k_pad = _pad(k, ((0, 0), (ks - 1, 0), (0, 0)))
        k_shift = torch.zeros_like(k)
        for t in range(ks):
            k_shift = k_shift + k_pad[:, t:t + S, :] * shift_k[:, ks - 1 - t][None, None, :]
        kv = k_shift * v
        L = 2 * S
        kv_pad = _pad(kv, ((0, 0), (0, S), (0, 0)))
        kf_pad = _pad(ssm_k, ((0, 0), (0, S)))
        kv_f = (lambda x, axes=(-2,-1): torch.fft.rfft2(x.float(), dim=axes))(kv_pad[:, None, :, :], axes=(1, 2))
        kf_f = (lambda x, axes=(-2,-1): torch.fft.rfft2(x.float(), dim=axes))(kf_pad[:, None, :], axes=(1, 2))
        kf_f = (lambda x, *d: x.permute(*d))(kf_f, 1, 2, 0)
        y_f = kv_f * kf_f[None, :, :, :]
        y = (lambda x, s, axes=(-2,-1): torch.fft.irfft2(x, s=s, dim=axes).to(torch.bfloat16))(y_f, (1, L), axes=(1, 2))[:, 0, :S, :]
        y = y + kv * ssm_D[None, None, :]
        return q * y

    def _B_ref(x, wq, wk, wv, wo):
        B, S, D = x.shape
        H = 32
        Dh = D // H
        q = (lambda x, *d: x.permute(*d))((x @ wq).reshape(B, S, H, Dh), 0, 2, 1, 3)
        k = x @ wk
        v = x @ wv
        scores = torch.einsum("bhqd,bkd->bhqk", q, k) * (Dh ** -0.5)
        p = _softmax(scores, axis=-1)
        o = torch.einsum("bhqk,bkd->bhqd", p, v)
        return (lambda x, *d: x.permute(*d))(o, 0, 2, 1, 3).reshape(B, S, H * Dh) @ wo

    a = _A_ref(A_q, A_k, A_v, A_shift_k, A_ssm_k, A_ssm_D)
    b = _B_ref(B_x, B_wq, B_wk, B_wv, B_wo)
    return torch.concatenate([a, b], axis=-1)
```

```cpp
#include <hip/hip_runtime.h>
#include <hip/hip_bf16.h>

typedef unsigned short u16;
typedef unsigned int u32;
typedef unsigned long long u64;
typedef __bf16 bf16x2 __attribute__((ext_vector_type(2)));
typedef __bf16 bf16x8 __attribute__((ext_vector_type(8)));
typedef __bf16 bf16x16 __attribute__((ext_vector_type(16)));
typedef short s16x4 __attribute__((ext_vector_type(4)));
typedef float f32x2 __attribute__((ext_vector_type(2)));
typedef float f32x4 __attribute__((ext_vector_type(4)));
typedef float f32x8 __attribute__((ext_vector_type(8)));
typedef float f32x16 __attribute__((ext_vector_type(16)));
typedef u32 u32x2 __attribute__((ext_vector_type(2)));
typedef u32 u32x4 __attribute__((ext_vector_type(4)));
typedef u32 u32x4u __attribute__((ext_vector_type(4), aligned(2)));
typedef __amdgpu_buffer_rsrc_t rsrc_t;
#define LAS __attribute__((address_space(3)))
#define GAS __attribute__((address_space(1)))
#define DI __device__ __forceinline__
#define SC1 16

constexpr int S = 2048, D = 640;
constexpr int NREP = 8;
constexpr u32 WS_E = 0;
constexpr u32 WS_FKV = 4096;
constexpr u32 WS_FA1 = WS_FKV + NREP * 2048;
constexpr u32 WS_FY = WS_FA1 + 2048;
constexpr u32 WS_FO = WS_FY + 8192;
constexpr u32 WS_DBG = WS_FO + 16384;
constexpr u32 WS_REC = 131072;
constexpr u32 WS_KVT = 524288;
constexpr u32 WS_YT = WS_KVT + D * S * 2;
constexpr u32 WS_O = WS_YT + D * S * 2;
constexpr u32 WS_END = WS_O + D * S * 2;
constexpr u32 L_W = 0;
constexpr u32 L_R2 = 102400;
constexpr u32 L_WK = L_R2;
constexpr u32 L_PART = L_R2 + 25664;
constexpr u32 L_XCH = L_R2;
constexpr u32 L_QIMG = L_R2 + 25664;
constexpr u32 L_ORED = L_R2;
constexpr u32 L_LRED = L_R2 + 36864;
constexpr u32 L_CX = L_R2 + 38912;
constexpr u32 L_CH = L_CX + 21504;
constexpr u32 L_CNT = L_CH + 576;
constexpr u32 L_END = 163840;
static_assert(L_CNT + 64 <= L_END, "lds");

#define STAMP(i) do {} while (0)

DI rsrc_t mkrs(const void* p, u32 bytes) { return __builtin_amdgcn_make_buffer_rsrc((void*)p, (short)0, (int)bytes, 0x00020000); }
DI u32 pk2(float a, float b) { f32x2 v = {a, b}; return __builtin_bit_cast(u32, __builtin_convertvector(v, bf16x2)); }
DI float lo2f(u32 x) { return __builtin_bit_cast(float, x << 16); }
DI float hi2f(u32 x) { return __builtin_bit_cast(float, x & 0xffff0000u); }
DI void lgkm0() { asm volatile("s_waitcnt lgkmcnt(0)" ::: "memory"); }
DI void vm0() { asm volatile("s_waitcnt vmcnt(0)" ::: "memory"); }
DI void bar() { asm volatile("s_waitcnt lgkmcnt(0)\n\ts_barrier" ::: "memory"); }
template <class T> DI LAS T* lds(u32 off) { return (LAS T*)(size_t)off; }
DI s16x4 trread(u32 addr) { return __builtin_bit_cast(s16x4, __builtin_amdgcn_ds_read_tr16_b64_v4i16((LAS s16x4*)(size_t)addr)); }
DI bf16x8 cat8(s16x4 a, s16x4 b) { return __builtin_bit_cast(bf16x8, __builtin_shufflevector(a, b, 0, 1, 2, 3, 4, 5, 6, 7)); }
DI void dma16(u32 lds_dst, u32 voff, rsrc_t rs, u32 soff) {
  u32 keep;
  asm volatile("s_mov_b32 %0, m0\n\ts_mov_b32 m0, %1\n\ts_nop 4\n\tbuffer_load_dwordx4 %2, %3, %4 offen lds\n\ts_mov_b32 m0, %0"
               : "=&s"(keep) : "s"(lds_dst), "v"(voff), "s"(rs), "s"(soff) : "memory");
}
DI u32 swap23(u32 x) { return (x & ~12u) | ((x & 4u) << 1) | ((x & 8u) >> 1); }

constexpr u32 SPIN_CAP = 1u << 17;
DI void wait_flags(const char* p, bool active, u64 tag) {
  for (u32 spins = 0; spins < SPIN_CAP; ++spins) {
    u64 v = tag;
    if (active) v = __hip_atomic_load((GAS u64*)p, __ATOMIC_RELAXED, __HIP_MEMORY_SCOPE_AGENT);
    if (__all(v == tag)) break;
    __builtin_amdgcn_s_sleep(1);
  }
  asm volatile("" ::: "memory");
}
DI void put_flag(char* p, u64 tag) { __hip_atomic_store((GAS u64*)p, tag, __ATOMIC_RELAXED, __HIP_MEMORY_SCOPE_AGENT); }
DI void lds_arrive(u32 cnt, int lane) {
  lgkm0();
  if (lane == 0) __hip_atomic_fetch_add(lds<u32>(cnt), 1u, __ATOMIC_RELAXED, __HIP_MEMORY_SCOPE_WORKGROUP);
}
DI void lds_wait(u32 cnt, u32 target) {
  for (u32 spins = 0; spins < (SPIN_CAP << 3); ++spins) {
    u32 v = __hip_atomic_load(lds<u32>(cnt), __ATOMIC_RELAXED, __HIP_MEMORY_SCOPE_WORKGROUP);
    if ((u32)__builtin_amdgcn_readfirstlane(v) >= target) break;
    __builtin_amdgcn_s_sleep(1);
  }
  asm volatile("" ::: "memory");
}

DI void stage_w_quarter(rsrc_t rs, int quarter, int hq, int lane) {
#pragma unroll
  for (int ph = 0; ph < 5; ++ph) {
    const u32 c = 64 * ph + lane, pos = c / 10, j = c - pos * 10;
    const u32 voff = swap23(pos) * 1280 + j * 16;
#pragma unroll
    for (int blk = 0; blk < 5; ++blk) {
      const u32 r0 = quarter * 160 + blk * 32;
      dma16(L_W + r0 * 160 + ph * 1024, voff, rs, r0 * 1280 + hq * 160);
    }
  }
}

DI void gemm_half(f32x4 (&acc)[5], const u32x4 (&xf)[10], int kh, int lane) {
  const int gq = lane >> 4, qq = (lane >> 2) & 3, p = lane & 3;
  const u32 base = L_W + kh * 51200 + (16 * (gq >> 1) + 4 * (gq & 1) + qq) * 160 + 8 * p;
#pragma unroll
  for (int ks = 0; ks < 10; ++ks) {
#pragma unroll
    for (int nt = 0; nt < 5; ++nt) {
      const bf16x8 wf = cat8(trread(base + ks * 5120 + nt * 32), trread(base + ks * 5120 + 1280 + nt * 32));
      acc[nt] = __builtin_amdgcn_mfma_f32_16x16x32_bf16(wf, __builtin_bit_cast(bf16x8, xf[ks]), acc[nt], 0, 0, 0);
    }
  }
}

template <int MASK, int NA>
DI void conv_task(rsrc_t rsH, rsrc_t rsW, u32 hoff, u32 ximg, u32 hhead, u32 yoff, int lane) {
  const int col = lane & 15, gq = lane >> 4;
  f32x4 acc[8];
#pragma unroll
  for (int i = 0; i < 8; ++i) acc[i] = (f32x4){0.f, 0.f, 0.f, 0.f};
  const u32 xb = ximg + 480 - 32 * col + 16 * gq;
  const u32 hv = 2 * (col + 8 * gq) + 34;
  const u32x4 a00 = *(LAS u32x4u*)(size_t)(hhead + 2 * (col + 8 * gq + 1));
#pragma unroll 1
  for (int b = 0; b < 8; ++b) {
    u32x4 af[NA], bf[NA];
#pragma unroll
    for (int a = 0; a < NA; ++a) {
      if (a == 0) af[a] = __builtin_amdgcn_raw_buffer_load_b128(rsH, hv, b == 0 ? hoff : hoff + 64 * b - 64, 0);
      else af[a] = __builtin_amdgcn_raw_buffer_load_b128(rsH, hv + (512 * a - 64), hoff + 64 * b, 0);
    }
#pragma unroll
    for (int c = 0; c < NA; ++c) bf[c] = *lds<u32x4>(xb + 512 * (7 - c) + 64 * b);
    if (b == 0) af[0] = a00;
#pragma unroll
    for (int a = 0; a < NA; ++a)
#pragma unroll
      for (int c = 0; c < NA; ++c)
        if (a + c < 8 && ((MASK >> (a + c)) & 1))
          acc[a + c] = __builtin_amdgcn_mfma_f32_16x16x32_bf16(__builtin_bit_cast(bf16x8, af[a]), __builtin_bit_cast(bf16x8, bf[c]), acc[a + c], 0, 0, 0);
  }
#pragma unroll
  for (int i = 0; i < 8; ++i)
    if ((MASK >> i) & 1) {
      u32x2 o = {pk2(acc[i][0], acc[i][1]), pk2(acc[i][2], acc[i][3])};
      __builtin_amdgcn_raw_buffer_store_b64(o, rsW, yoff + 32 * col + 8 * gq + 512 * i, 0, SC1);
    }
}

struct Rec { u32x4 k1a, k1b, k2, v0, v1, v2, v3; };
DI void load_rec(Rec& r, rsrc_t rsW, u32 voff, u32 soff) {
  r.k1a = __builtin_amdgcn_raw_buffer_load_b128(rsW, voff, soff, SC1);
  r.k2 = __builtin_amdgcn_raw_buffer_load_b128(rsW, voff + 2048, soff, SC1);
  r.v0 = __builtin_amdgcn_raw_buffer_load_b128(rsW, voff + 3072, soff, SC1);
  r.v1 = __builtin_amdgcn_raw_buffer_load_b128(rsW, voff, soff + 4096, SC1);
  r.k1b = __builtin_amdgcn_raw_buffer_load_b128(rsW, voff + 1024, soff, SC1);
  r.v2 = __builtin_amdgcn_raw_buffer_load_b128(rsW, voff + 1024, soff + 4096, SC1);
  r.v3 = __builtin_amdgcn_raw_buffer_load_b128(rsW, voff + 2048, soff + 4096, SC1);
}
#define SB() __builtin_amdgcn_sched_barrier(0)
DI f32x16 qk16(u32x4 k1, u32x4 q1) {
  f32x16 z;
#pragma unroll
  for (int i = 0; i < 16; ++i) z[i] = 0.f;
  return __builtin_amdgcn_mfma_f32_32x32x16_bf16(__builtin_bit_cast(bf16x8, k1), __builtin_bit_cast(bf16x8, q1), z, 0, 0, 0);
}
DI f32x16 qk8(u32x2 k2, u32x2 q2, f32x16 s) {
  return __builtin_amdgcn_mfma_f32_32x32x8bf16_1k(__builtin_bit_cast(s16x4, k2), __builtin_bit_cast(s16x4, q2), s, 0, 0, 0);
}
DI f32x16 pv_lo(u32x4 v, f32x16 p, f32x16 o, int idx) {
  f32x8 h = __builtin_shufflevector(p, p, 0, 1, 2, 3, 4, 5, 6, 7);
  return __builtin_amdgcn_smfmac_f32_32x32x32_bf16(__builtin_bit_cast(bf16x8, v), __builtin_bit_cast(bf16x16, h), o, idx, 0, 0);
}
DI f32x16 pv_hi(u32x4 v, f32x16 p, f32x16 o, int idx) {
  f32x8 h = __builtin_shufflevector(p, p, 8, 9, 10, 11, 12, 13, 14, 15);
  return __builtin_amdgcn_smfmac_f32_32x32x32_bf16(__builtin_bit_cast(bf16x8, v), __builtin_bit_cast(bf16x16, h), o, idx, 0, 0);
}
#define EXP4(X, i) do { X[i] = __builtin_amdgcn_exp2f(X[i]); X[i + 1] = __builtin_amdgcn_exp2f(X[i + 1]); X[i + 2] = __builtin_amdgcn_exp2f(X[i + 2]); X[i + 3] = __builtin_amdgcn_exp2f(X[i + 3]); } while (0)
#define STEP(H, VPREV, VCUR, K1N, K2N) do { \
    EXP4(Sx[H], 0); SB(); \
    O[(H + 3) & 3] = pv_hi(VPREV, Sx[(H + 3) & 3], O[(H + 3) & 3], idx); SB(); \
    EXP4(Sx[H], 4); SB(); \
    Sx[(H + 2) & 3] = qk16(K1N, q1[(H + 2) & 3]); SB(); \
    EXP4(Sx[H], 8); SB(); \
    Sx[(H + 2) & 3] = qk8(K2N, q2[(H + 2) & 3], Sx[(H + 2) & 3]); SB(); \
    EXP4(Sx[H], 12); SB(); \
    O[H] = pv_lo(VCUR, Sx[H], O[H], idx); SB(); \
  } while (0)
DI u32x2 klo(u32x4 k) { return __builtin_shufflevector(k, k, 0, 1); }
DI u32x2 khi(u32x4 k) { return __builtin_shufflevector(k, k, 2, 3); }
#define REC_STEPS(R, VP, N) do { \
    STEP(0, VP, R.v0, R.k1a, klo(R.k2)); \
    STEP(1, R.v1, R.v0, R.k1a, klo(R.k2)); \
    STEP(2, R.v1, R.v0, R.k1b, khi(R.k2)); \
    STEP(3, R.v1, R.v0, R.k1b, khi(R.k2)); \
    STEP(0, R.v1, R.v2, R.k1b, khi(R.k2)); \
    STEP(1, R.v3, R.v2, R.k1b, khi(R.k2)); \
    STEP(2, R.v3, R.v2, N.k1a, klo(N.k2)); \
    STEP(3, R.v3, R.v2, N.k1a, klo(N.k2)); \
  } while (0)

__global__ void __launch_bounds__(512, 2)
fused_kernel(const u16* __restrict__ Aq, const u16* __restrict__ Ak, const u16* __restrict__ Av, const u16* __restrict__ Ash,
             const u16* __restrict__ Assm, const u16* __restrict__ AD, const u16* __restrict__ Bx, const u16* __restrict__ Bwq,
             const u16* __restrict__ Bwk, const u16* __restrict__ Bwv, const u16* __restrict__ Bwo, u16* __restrict__ out, char* ws) {
  __shared__ __attribute__((aligned(16))) char smem[L_END];
  asm volatile("" ::"v"((u32)(size_t)(LAS char*)smem));
  const int lane = threadIdx.x & 63;
  const int w = __builtin_amdgcn_readfirstlane(threadIdx.x >> 6);
  const int bj = blockIdx.x >> 3, g = (blockIdx.x & 7) * 4 + (bj >> 3), hq = bj & 7;
  const int gq = lane >> 4, l15 = lane & 15;
  const rsrc_t rsW = mkrs(ws, WS_END), rsX = mkrs(Bx, S * D * 2);
  STAMP(0);

  u32x4 xf[10];
  const u32 xvoff = (64 * g + 16 * (w >> 1) + l15) * 1280 + (w & 1) * 640 + gq * 16;
  if (w < 4) {
    if (threadIdx.x < 8) *lds<u32>(L_CNT + threadIdx.x * 4) = 0;
    bar();
#pragma unroll
    for (int ks = 0; ks < 10; ++ks) xf[ks] = __builtin_amdgcn_raw_buffer_load_b128(rsX, xvoff + ks * 64, 0, 0);
    stage_w_quarter(mkrs(Bwq, D * D * 2), w, hq, lane);
    vm0();
  } else {
    const int kw = w - 4;
    const u32x2 ep = __builtin_amdgcn_raw_buffer_load_b64(rsW, w == 7 ? WS_E : 0x80000000u, 0, SC1);
    const rsrc_t rsK = mkrs((hq & 1) ? Bwv : Bwk, D * 20 * 2);
#pragma unroll
    for (int i = 0; i < 6; ++i) dma16(L_WK + kw * 6400 + i * 1024, lane * 16, rsK, kw * 6400 + i * 1024);
    if (lane < 16) dma16(L_WK + kw * 6400 + 6144, lane * 16, rsK, kw * 6400 + 6144);
    u32x4 xk[5];
    const u32 xkoff = (64 * g + 16 * (hq >> 1) + l15) * 1280 + kw * 320 + gq * 16;
#pragma unroll
    for (int ks = 0; ks < 5; ++ks) xk[ks] = __builtin_amdgcn_raw_buffer_load_b128(rsX, xkoff + ks * 64, 0, 0);
    const int item = kw * 64 + lane, run = item / 20, cq = item - run * 20;
    const bool a1 = item < 160;
    u32x2 kr[11], vr[8];
    u32x4 sw0, sw1;
    {
      const rsrc_t rsAk = mkrs(Ak, S * D * 2), rsAv = mkrs(Av, S * D * 2), rsSh = mkrs(Ash, D * 4 * 2);
      const int t0 = 64 * g + 8 * run;
      const u32 cb = hq * 160 + cq * 8;
#pragma unroll
      for (int i = 0; i < 11; ++i) {
        const int t = t0 + i - 3;
        kr[i] = __builtin_amdgcn_raw_buffer_load_b64(rsAk, (a1 && t >= 0) ? (u32)t * 1280 + cb : 0x80000000u, 0, 0);
      }
#pragma unroll
      for (int i = 0; i < 8; ++i) vr[i] = __builtin_amdgcn_raw_buffer_load_b64(rsAv, a1 ? (u32)(t0 + i) * 1280 + cb : 0x80000000u, 0, 0);
      sw0 = __builtin_amdgcn_raw_buffer_load_b128(rsSh, a1 ? cb * 4 : 0x80000000u, 0, 0);
      sw1 = __builtin_amdgcn_raw_buffer_load_b128(rsSh, a1 ? cb * 4 + 16 : 0x80000000u, 0, 0);
    }
    bar();
#pragma unroll
    for (int ks = 0; ks < 10; ++ks) xf[ks] = __builtin_amdgcn_raw_buffer_load_b128(rsX, xvoff + ks * 64, 0, 0);
    asm volatile("" : "+v"(xk[4])::"memory");
    {
      const u32 rb = L_WK + kw * 6400 + (8 * gq + ((lane >> 2) & 3)) * 40 + 8 * (lane & 3);
      f32x4 a0 = {0.f, 0.f, 0.f, 0.f}, a1v = a0;
      bf16x8 w0[5], w1[5];
#pragma unroll
      for (int ks = 0; ks < 5; ++ks) {
        w0[ks] = cat8(trread(rb + ks * 1280), trread(rb + ks * 1280 + 160));
        w1[ks] = cat8(trread(rb + ks * 1280 + 32), trread(rb + ks * 1280 + 192));
      }
      if (hq & 1) {
#pragma unroll
        for (int ks = 0; ks < 5; ++ks) {
          a0 = __builtin_amdgcn_mfma_f32_16x16x32_bf16(__builtin_bit_cast(bf16x8, xk[ks]), w0[ks], a0, 0, 0, 0);
          a1v = __builtin_amdgcn_mfma_f32_16x16x32_bf16(__builtin_bit_cast(bf16x8, xk[ks]), w1[ks], a1v, 0, 0, 0);
        }
      } else {
#pragma unroll
        for (int ks = 0; ks < 5; ++ks) {
          a0 = __builtin_amdgcn_mfma_f32_16x16x32_bf16(w0[ks], __builtin_bit_cast(bf16x8, xk[ks]), a0, 0, 0, 0);
          a1v = __builtin_amdgcn_mfma_f32_16x16x32_bf16(w1[ks], __builtin_bit_cast(bf16x8, xk[ks]), a1v, 0, 0, 0);
        }
      }
      if (w < 7) {
        *lds<f32x4>(L_PART + (kw * 2) * 1024 + lane * 16) = a0;
        *lds<f32x4>(L_PART + (kw * 2 + 1) * 1024 + lane * 16) = a1v;
        lds_arrive(L_CNT + 0, lane);
      } else {
        lds_wait(L_CNT + 0, 3);
#pragma unroll
        for (int i = 0; i < 3; ++i) {
          a0 += *lds<f32x4>(L_PART + (i * 2) * 1024 + lane * 16);
          a1v += *lds<f32x4>(L_PART + (i * 2 + 1) * 1024 + lane * 16);
        }
        u32x2 o0 = {pk2(a0[0], a0[1]), pk2(a0[2], a0[3])}, o1 = {pk2(a1v[0], a1v[1]), pk2(a1v[2], a1v[3])};
        const int rbk = hq >> 1;
        const u32 rec = WS_REC + g * 7168;
        if (hq & 1) {
          if (l15 == 4) o1 = (u32x2){0x3f803f80u, 0x3f803f80u};
          if (l15 > 4) o1 = (u32x2){0u, 0u};
          const u32 vb = 3072 + rbk * 1024 + (gq >> 1) * 512 + (gq & 1) * 8;
          __builtin_amdgcn_raw_buffer_store_b64(o0, rsW, rec + vb + swap23(l15) * 16, 0, SC1);
          __builtin_amdgcn_raw_buffer_store_b64(o1, rsW, rec + vb + (16 + l15) * 16, 0, SC1);
        } else {
          const float c = 0.22360679774997896f * 1.4426950408889634f;
          o0 = (u32x2){pk2(lo2f(o0[0]) * c, hi2f(o0[0]) * c), pk2(lo2f(o0[1]) * c, hi2f(o0[1]) * c)};
          o1 = (u32x2){pk2(lo2f(o1[0]) * c, hi2f(o1[0]) * c), pk2(lo2f(o1[1]) * c, hi2f(o1[1]) * c)};
          if (gq == 1) o1 = (u32x2){0x3f803f80u, 0x00003f80u};
          const u32 T = rbk >> 1, key = 16 * (rbk & 1) + l15;
          __builtin_amdgcn_raw_buffer_store_b64(o0, rsW, rec + T * 1024 + (key + 32 * (gq >> 1)) * 16 + (gq & 1) * 8, 0, SC1);
          __builtin_amdgcn_raw_buffer_store_b64(o1, rsW, gq < 2 ? rec + 2048 + (key + 32 * gq) * 16 + T * 8 : 0x80000000u, 0, SC1);
        }
        const u64 tag7 = ((u64)ep[0] | ((u64)ep[1] << 32)) + 0x9E3779B97F4A7C15ull;
        vm0();
        if (lane < NREP) put_flag(ws + WS_FKV + lane * 2048 + (g * 8 + hq) * 8, tag7);
        if (lane == 0) *lds<u64>(L_CNT + 32) = tag7;
        STAMP(1);
      }
    }
    if (w < 7) {
      u32x4 res[4];
#pragma unroll
      for (int c = 0; c < 4; ++c) {
        const u32 wa = (c < 2) ? sw0[2 * (c & 1)] : sw1[2 * (c & 1)], wb = (c < 2) ? sw0[2 * (c & 1) + 1] : sw1[2 * (c & 1) + 1];
        const float w0 = lo2f(wa), w1 = hi2f(wa), w2 = lo2f(wb), w3 = hi2f(wb);
        float kf[11], r[8];
#pragma unroll
        for (int i = 0; i < 11; ++i) kf[i] = (c & 1) ? hi2f(kr[i][c >> 1]) : lo2f(kr[i][c >> 1]);
#pragma unroll
        for (int i = 0; i < 8; i += 2) {
          u32 p0 = pk2(kf[i] * w3, kf[i + 1] * w3), p1 = pk2(kf[i + 1] * w2, kf[i + 2] * w2);
          u32 p2 = pk2(kf[i + 2] * w1, kf[i + 3] * w1), p3 = pk2(kf[i + 3] * w0, kf[i + 4] * w0);
          u32 s = pk2(lo2f(p0) + lo2f(p1), hi2f(p0) + hi2f(p1));
          s = pk2(lo2f(s) + lo2f(p2), hi2f(s) + hi2f(p2));
          s = pk2(lo2f(s) + lo2f(p3), hi2f(s) + hi2f(p3));
          const float v0 = (c & 1) ? hi2f(vr[i][c >> 1]) : lo2f(vr[i][c >> 1]);
          const float v1 = (c & 1) ? hi2f(vr[i + 1][c >> 1]) : lo2f(vr[i + 1][c >> 1]);
          r[i] = lo2f(s) * v0;
          r[i + 1] = hi2f(s) * v1;
        }
        res[c] = (u32x4){pk2(r[7], r[6]), pk2(r[5], r[4]), pk2(r[3], r[2]), pk2(r[1], r[0])};
      }
      const u32 so = WS_KVT + (hq * 80 + cq * 4) * 4096 + (2040 - 64 * g - 8 * run) * 2;
#pragma unroll
      for (int c = 0; c < 4; ++c) __builtin_amdgcn_raw_buffer_store_b128(res[c], rsW, a1 ? so + c * 4096 : 0x80000000u, 0, SC1);
    }
  }
  STAMP(2);
  bar();
  u64 tag;
  {
    const u32x2 tv = *lds<u32x2>(L_CNT + 32);
    tag = (u64)(u32)__builtin_amdgcn_readfirstlane(tv[0]) | ((u64)(u32)__builtin_amdgcn_readfirstlane(tv[1]) << 32);
  }
  STAMP(3);
  {
    f32x4 acc[5];
#pragma unroll
    for (int i = 0; i < 5; ++i) acc[i] = (f32x4){0.f, 0.f, 0.f, 0.f};
    gemm_half(acc, xf, w & 1, lane);
    const u32 xb = L_XCH + (w >> 1) * 5120 + lane * 16;
    if (w & 1) {
#pragma unroll
      for (int i = 0; i < 3; ++i) *lds<f32x4>(xb + i * 1024) = acc[i];
    } else {
#pragma unroll
      for (int i = 3; i < 5; ++i) *lds<f32x4>(xb + i * 1024) = acc[i];
    }
    vm0();
    bar();
    if (threadIdx.x == 0) put_flag(ws + WS_FA1 + (hq * 32 + g) * 8, tag);
    const u32 qb = L_QIMG + (16 * (w >> 1) + l15) * 168 + gq * 8;
    if (w & 1) {
#pragma unroll
      for (int i = 3; i < 5; ++i) {
        const f32x4 v = acc[i] + *lds<f32x4>(xb + i * 1024);
        *lds<u32x2>(qb + i * 32) = (u32x2){pk2(v[0], v[1]), pk2(v[2], v[3])};
      }
    } else {
#pragma unroll
      for (int i = 0; i < 3; ++i) {
        const f32x4 v = acc[i] + *lds<f32x4>(xb + i * 1024);
        *lds<u32x2>(qb + i * 32) = (u32x2){pk2(v[0], v[1]), pk2(v[2], v[3])};
      }
    }
  }
  bar();
  const int half = w >> 2, wq4 = w & 3, h = lane >> 5, l31 = lane & 31;
  u32x4 q1[4];
  u32x2 q2[4];
#pragma unroll
  for (int hd = 0; hd < 4; ++hd) {
    const u32 qa = L_QIMG + (32 * half + l31) * 168 + hd * 40;
    const u32x2 a = *lds<u32x2>(qa + 16 * h), b = *lds<u32x2>(qa + 16 * h + 8), c = *lds<u32x2>(qa + 32);
    q1[hd] = (u32x4){a[0], a[1], b[0], b[1]};
    q2[hd] = h ? (u32x2){0u, 0u} : c;
  }
  bar();
  STAMP(4);

  if (half == 1) {
    if (g < 27) {
      const rsrc_t rsH = mkrs(Assm, D * S * 2);
      const int ch0 = hq * 80 + 3 * g, nch = (g == 26) ? 2 : 3;
      wait_flags(ws + WS_FA1 + (hq * 32 + l31) * 8, true, tag);
      STAMP(5);
      const u32x4 z4 = {0u, 0u, 0u, 0u};
      if (wq4 < 3) {
        if (wq4 < nch) {
          const int ch = ch0 + wq4;
          const u32 img = L_CX + wq4 * 4608, hh = L_CH + wq4 * 96;
          u32x4 t[4];
#pragma unroll
          for (int i = 0; i < 4; ++i) t[i] = __builtin_amdgcn_raw_buffer_load_b128(rsW, lane * 16 + i * 1024, WS_KVT + ch * 4096, SC1);
          const u32x4 hd = __builtin_amdgcn_raw_buffer_load_b128(rsH, lane >= 2 ? (lane - 2) * 16 : 0x80000000u, ch * 4096, 0);
#pragma unroll
          for (int i = 0; i < 4; ++i) *lds<u32x4>(img + lane * 16 + i * 1024) = t[i];
          if (lane < 32) *lds<u32x4>(img + 4096 + lane * 16) = z4;
          if (lane < 6) *lds<u32x4>(hh + lane * 16) = hd;
          lgkm0();
          conv_task<0xF1, 8>(rsH, rsW, ch * 4096, img, hh, WS_YT + ch * 4096, lane);
        }
      } else {
#pragma unroll 1
        for (int i = 0; i < nch; ++i) {
          const int ch = ch0 + i;
          const u32 img = L_CX + 13824 + i * 2560, hh = L_CH + (3 + i) * 96;
          const u32x4 t0 = __builtin_amdgcn_raw_buffer_load_b128(rsW, lane * 16 + 2048, WS_KVT + ch * 4096, SC1);
          const u32x4 t1 = __builtin_amdgcn_raw_buffer_load_b128(rsW, lane * 16 + 3072, WS_KVT + ch * 4096, SC1);
          const u32x4 hd = __builtin_amdgcn_raw_buffer_load_b128(rsH, lane >= 2 ? (lane - 2) * 16 : 0x80000000u, ch * 4096, 0);
          *lds<u32x4>(img + lane * 16) = t0;
          *lds<u32x4>(img + 1024 + lane * 16) = t1;
          if (lane < 32) *lds<u32x4>(img + 2048 + lane * 16) = z4;
          if (lane < 6) *lds<u32x4>(hh + lane * 16) = hd;
          lgkm0();
          conv_task<0x0E, 4>(rsH, rsW, ch * 4096, img - 2048, hh, WS_YT + ch * 4096, lane);
        }
      }
      vm0();
      if (lane == 0) put_flag(ws + WS_FY + ((hq * 32 + g) * 4 + wq4) * 8, tag);
    }
    STAMP(6);
    stage_w_quarter(mkrs(Bwo, D * D * 2), wq4, hq, lane);
    vm0();
  }
  wait_flags(ws + WS_FKV + (blockIdx.x & (NREP - 1)) * 2048 + (wq4 * 64 + lane) * 8, true, tag);
  STAMP(7);
  {
    const int idx = (int)0xDDDDDDDDu;
    const u32 voff = lane * 16, rec0 = WS_REC + wq4 * 8 * 7168;
    const u32 cntA = L_CNT + 4 + half * 4;
    u32 round = 0;
    f32x16 O[4];
    float lsum;
#pragma unroll 1
    for (int attempt = 0; attempt < 2; ++attempt) {
      if (attempt == 1) {
        float m[4] = {-INFINITY, -INFINITY, -INFINITY, -INFINITY};
#pragma unroll 1
        for (int r = 0; r < 8; ++r) {
          const u32x4 k1a = __builtin_amdgcn_raw_buffer_load_b128(rsW, voff, rec0 + r * 7168, SC1);
          const u32x4 k1b = __builtin_amdgcn_raw_buffer_load_b128(rsW, voff + 1024, rec0 + r * 7168, SC1);
          const u32x4 k2 = __builtin_amdgcn_raw_buffer_load_b128(rsW, voff + 2048, rec0 + r * 7168, SC1);
#pragma unroll
          for (int hd = 0; hd < 4; ++hd) {
            const f32x16 sa = qk8(klo(k2), q2[hd], qk16(k1a, q1[hd])), sb = qk8(khi(k2), q2[hd], qk16(k1b, q1[hd]));
#pragma unroll
            for (int i = 0; i < 16; ++i) m[hd] = fmaxf(m[hd], fmaxf(sa[i], sb[i]));
          }
        }
#pragma unroll
        for (int hd = 0; hd < 4; ++hd) *lds<float>(L_ORED + (((l31 * 4 + hd) * 4 + wq4) * 2 + h) * 4) = m[hd];
        lds_arrive(cntA, lane);
        lds_wait(cntA, 4 * (++round));
#pragma unroll
        for (int hd = 0; hd < 4; ++hd) {
          const f32x4 a = *lds<f32x4>(L_ORED + (l31 * 4 + hd) * 32), b = *lds<f32x4>(L_ORED + (l31 * 4 + hd) * 32 + 16);
          const float M = -fmaxf(fmaxf(fmaxf(a[0], a[1]), fmaxf(a[2], a[3])), fmaxf(fmaxf(b[0], b[1]), fmaxf(b[2], b[3])));
          const u32 m0 = __builtin_bit_cast(u32, M) & 0xffff0000u;
          const float r1 = M - __builtin_bit_cast(float, m0);
          const u32 m1 = __builtin_bit_cast(u32, r1) & 0xffff0000u;
          const float r2 = r1 - __builtin_bit_cast(float, m1);
          if (h) q2[hd] = (u32x2){(m0 >> 16) | m1, __builtin_bit_cast(u32, r2) >> 16};
        }
        lds_arrive(cntA, lane);
        lds_wait(cntA, 4 * (++round));
      }
      f32x16 Sx[4];
#pragma unroll
      for (int i = 0; i < 4; ++i)
#pragma unroll
        for (int j = 0; j < 16; ++j) { O[i][j] = 0.f; Sx[i][j] = 0.f; }
      Rec A, B;
      load_rec(A, rsW, voff, rec0);
      B.v3 = (u32x4){0u, 0u, 0u, 0u};
      Sx[0] = qk8(klo(A.k2), q2[0], qk16(A.k1a, q1[0]));
      Sx[1] = qk8(klo(A.k2), q2[1], qk16(A.k1a, q1[1]));
#pragma unroll 1
      for (int r = 0; r < 8; r += 2) {
        const u32x4 vpa = B.v3;
        load_rec(B, rsW, voff, rec0 + (r + 1) * 7168);
        SB();
        REC_STEPS(A, vpa, B);
        const u32x4 vpb = A.v3;
        load_rec(A, rsW, voff, rec0 + ((r + 2) & 7) * 7168);
        SB();
        REC_STEPS(B, vpb, A);
      }
      O[3] = pv_hi(B.v3, Sx[3], O[3], idx);
      STAMP(8);
      if (half == 1 && attempt == 0) lds_wait(L_CNT + 12, 4);
#pragma unroll
      for (int hd = 0; hd < 4; ++hd) {
        if (hd != wq4) {
          const u32 slot = hd * 3 + (wq4 > hd ? wq4 - 1 : wq4);
#pragma unroll
          for (int i = 0; i < 3; ++i)
            *lds<f32x4>(L_ORED + (slot * 3 + i) * 1024 + lane * 16) = (f32x4){O[hd][4 * i], O[hd][4 * i + 1], O[hd][4 * i + 2], O[hd][4 * i + 3]};
        }
        if (h) *lds<float>(L_LRED + ((l31 * 4 + hd) * 4 + wq4) * 4) = O[hd][8];
      }
      lds_arrive(cntA, lane);
      lds_wait(cntA, 4 * (++round));
      const f32x4 la = *lds<f32x4>(L_LRED + (l31 * 4 + 2 * h) * 16), lb = *lds<f32x4>(L_LRED + (l31 * 4 + 2 * h + 1) * 16);
      const f32x4 lo = *lds<f32x4>(L_LRED + (l31 * 4 + wq4) * 16);
      const float sa = (la[0] + la[1]) + (la[2] + la[3]), sb = (lb[0] + lb[1]) + (lb[2] + lb[3]);
      lsum = (lo[0] + lo[1]) + (lo[2] + lo[3]);
      const float LO = 5.421010862427522e-20f, HI = 1.8446744073709552e19f;
      const bool ok = sa >= LO && sa <= HI && sb >= LO && sb <= HI;
      if (__all(ok) || attempt == 1) break;
      lds_arrive(cntA, lane);
      lds_wait(cntA, 4 * (++round));
    }
    f32x4 t[3];
#pragma unroll
    for (int hd = 0; hd < 4; ++hd)
      if (hd == wq4) {
#pragma unroll
        for (int i = 0; i < 3; ++i) t[i] = (f32x4){O[hd][4 * i], O[hd][4 * i + 1], O[hd][4 * i + 2], O[hd][4 * i + 3]};
      }
#pragma unroll
    for (int s = 0; s < 3; ++s)
#pragma unroll
      for (int i = 0; i < 3; ++i) t[i] += *lds<f32x4>(L_ORED + ((wq4 * 3 + s) * 3 + i) * 1024 + lane * 16);
    if (half == 0) lds_arrive(L_CNT + 12, lane);
    const float inv = 1.0f / lsum;
    const u32x4 o0 = {pk2(t[0][0] * inv, t[0][1] * inv), pk2(t[0][2] * inv, t[0][3] * inv), pk2(t[1][0] * inv, t[1][1] * inv), pk2(t[1][2] * inv, t[1][3] * inv)};
    const u32x2 o1 = {pk2(t[2][0] * inv, t[2][1] * inv), pk2(t[2][2] * inv, t[2][3] * inv)};
    const u32 oo = WS_O + (64 * g + 32 * half + l31) * 1280 + hq * 160 + wq4 * 40;
    __builtin_amdgcn_raw_buffer_store_b128(o0, rsW, oo + 16 * h, 0, SC1);
    __builtin_amdgcn_raw_buffer_store_b64(o1, rsW, h ? 0x80000000u : oo + 32, 0, SC1);
    vm0();
    if (lane == 0) put_flag(ws + WS_FO + (((g * 2 + half) * 8 + hq) * 4 + wq4) * 8, tag);
    STAMP(9);
  }

  const rsrc_t rsO = mkrs(out, S * 2 * D * 2);
  if (w < 3) {
    const int item = w * 64 + lane, run = item / 20, cq = item - run * 20;
    const bool a3 = item < 160;
    const int ti = g >> 2;
    const bool small = ti >= 1 && ti <= 3;
    {
      const int p0 = lane, p1 = lane + 64;
      wait_flags(ws + WS_FY + (hq * 128 + p0) * 8, small ? (p0 & 3) == 3 : (p0 & 3) != 3, tag);
      wait_flags(ws + WS_FY + (hq * 128 + p1) * 8, p1 < 108 && (small ? (p1 & 3) == 3 : (p1 & 3) != 3), tag);
    }
    const rsrc_t rsAq = mkrs(Aq, S * D * 2), rsD = mkrs(AD, D * 2);
    const int t0 = 64 * g + 8 * run;
    const u32 cb = hq * 160 + cq * 8;
    u32x4 yv[4], kvv[4];
    u32x2 qr[8];
#pragma unroll
    for (int c = 0; c < 4; ++c) {
      yv[c] = __builtin_amdgcn_raw_buffer_load_b128(rsW, a3 ? (hq * 80 + cq * 4 + c) * 4096 + t0 * 2 : 0x80000000u, WS_YT, SC1);
      kvv[c] = __builtin_amdgcn_raw_buffer_load_b128(rsW, a3 ? (hq * 80 + cq * 4 + c) * 4096 + (2040 - t0) * 2 : 0x80000000u, WS_KVT, SC1);
    }
#pragma unroll
    for (int i = 0; i < 8; ++i) qr[i] = __builtin_amdgcn_raw_buffer_load_b64(rsAq, a3 ? (u32)(t0 + i) * 1280 + cb : 0x80000000u, 0, 0);
    const u32x2 dd = __builtin_amdgcn_raw_buffer_load_b64(rsD, a3 ? cb : 0x80000000u, 0, 0);
    float r[4][8];
#pragma unroll
    for (int c = 0; c < 4; ++c) {
      const float dc = (c & 1) ? hi2f(dd[c >> 1]) : lo2f(dd[c >> 1]);
#pragma unroll
      for (int i = 0; i < 8; i += 2) {
        const u32 kp = kvv[c][3 - (i >> 1)];
        const u32 m = pk2(hi2f(kp) * dc, lo2f(kp) * dc);
        const u32 y2 = pk2(lo2f(yv[c][i >> 1]) + lo2f(m), hi2f(yv[c][i >> 1]) + hi2f(m));
        r[c][i] = lo2f(y2) * ((c & 1) ? hi2f(qr[i][c >> 1]) : lo2f(qr[i][c >> 1]));
        r[c][i + 1] = hi2f(y2) * ((c & 1) ? hi2f(qr[i + 1][c >> 1]) : lo2f(qr[i + 1][c >> 1]));
      }
    }
#pragma unroll
    for (int i = 0; i < 8; ++i)
      __builtin_amdgcn_raw_buffer_store_b64((u32x2){pk2(r[0][i], r[1][i]), pk2(r[2][i], r[3][i])}, rsO, a3 ? (u32)(t0 + i) * 2560 + cb : 0x80000000u, 0, 0);
  }
  STAMP(10);
  bar();
  if (blockIdx.x == 0 && threadIdx.x == 0) put_flag(ws + WS_E, tag);
  {
    wait_flags(ws + WS_FO + (((g * 2 + (w >> 2)) * 8 + (w & 1) * 4) * 4 + l15) * 8, true, tag);
    STAMP(11);
#pragma unroll
    for (int ks = 0; ks < 10; ++ks) xf[ks] = __builtin_amdgcn_raw_buffer_load_b128(rsW, xvoff + ks * 64, WS_O, SC1);
    f32x4 acc[5];
#pragma unroll
    for (int i = 0; i < 5; ++i) acc[i] = (f32x4){0.f, 0.f, 0.f, 0.f};
    gemm_half(acc, xf, w & 1, lane);
    const u32 xb = L_XCH + (w >> 1) * 5120 + lane * 16;
    if (w & 1) {
#pragma unroll
      for (int i = 0; i < 3; ++i) *lds<f32x4>(xb + i * 1024) = acc[i];
    } else {
#pragma unroll
      for (int i = 3; i < 5; ++i) *lds<f32x4>(xb + i * 1024) = acc[i];
    }
    bar();
    const u32 ob = (64 * g + 16 * (w >> 1) + l15) * 2560 + 1280 + hq * 160 + gq * 8;
    if (w & 1) {
#pragma unroll
      for (int i = 3; i < 5; ++i) {
        const f32x4 v = acc[i] + *lds<f32x4>(xb + i * 1024);
        __builtin_amdgcn_raw_buffer_store_b64((u32x2){pk2(v[0], v[1]), pk2(v[2], v[3])}, rsO, ob + i * 32, 0, 0);
      }
    } else {
#pragma unroll
      for (int i = 0; i < 3; ++i) {
        const f32x4 v = acc[i] + *lds<f32x4>(xb + i * 1024);
        __builtin_amdgcn_raw_buffer_store_b64((u32x2){pk2(v[0], v[1]), pk2(v[2], v[3])}, rsO, ob + i * 32, 0, 0);
      }
    }
  }
  STAMP(12);
}

extern "C" void kernel_launch(void* const* d_in, const int* in_sizes, int n_in, void* d_out, int out_size, void* d_ws, size_t ws_size,
                              hipStream_t stream) {
  fused_kernel<<<256, 512, 0, stream>>>((const u16*)d_in[0], (const u16*)d_in[1], (const u16*)d_in[2], (const u16*)d_in[3],
                                        (const u16*)d_in[4], (const u16*)d_in[5], (const u16*)d_in[6], (const u16*)d_in[7],
                                        (const u16*)d_in[8], (const u16*)d_in[9], (const u16*)d_in[10], (u16*)d_out, (char*)d_ws);
}
```

```cpp
#include <hip/hip_runtime.h>
#include <hip/hip_bf16.h>

typedef unsigned short u16;
typedef unsigned int u32;
typedef unsigned long long u64;
typedef __bf16 bf16x2 __attribute__((ext_vector_type(2)));
typedef __bf16 bf16x8 __attribute__((ext_vector_type(8)));
typedef __bf16 bf16x16 __attribute__((ext_vector_type(16)));
typedef short s16x4 __attribute__((ext_vector_type(4)));
typedef float f32x2 __attribute__((ext_vector_type(2)));
typedef float f32x4 __attribute__((ext_vector_type(4)));
typedef float f32x8 __attribute__((ext_vector_type(8)));
typedef float f32x16 __attribute__((ext_vector_type(16)));
typedef u32 u32x2 __attribute__((ext_vector_type(2)));
typedef u32 u32x4 __attribute__((ext_vector_type(4)));
typedef u32 u32x4u __attribute__((ext_vector_type(4), aligned(2)));
typedef __amdgpu_buffer_rsrc_t rsrc_t;
#define LAS __attribute__((address_space(3)))
#define GAS __attribute__((address_space(1)))
#define DI __device__ __forceinline__
#define SC1 16
#define NT 2
#define OOB 0x80000000u
#define SB() __builtin_amdgcn_sched_barrier(0)

constexpr int S = 2048, D = 640;
constexpr int NREP = 8;
constexpr u32 WS_E = 0;
constexpr u32 WS_FKV = 4096;
constexpr u32 WS_FA1 = WS_FKV + NREP * 2048;
constexpr u32 WS_FY = WS_FA1 + 8192;
constexpr u32 WS_FO = WS_FY + 8192;
constexpr u32 WS_DBG = WS_FO + 16384;
constexpr u32 WS_REC = 131072;
constexpr u32 WS_KVT = 524288;
constexpr u32 WS_YT = WS_KVT + D * S * 2;
constexpr u32 WS_O = WS_YT + D * S * 2;
constexpr u32 WS_END = WS_O + D * S * 2;
constexpr u32 L_X = 0;
constexpr u32 L_RING = 84480;
constexpr u32 L_WK = L_RING + 40960;
constexpr u32 L_PART = L_WK + 25600;
constexpr u32 L_QF = 145920;
constexpr u32 CV_CH = 21504;
constexpr u32 L_CV = 0;
constexpr u32 L_CACC = 3 * CV_CH;
constexpr u32 L_A1 = L_CACC;
constexpr u32 L_KV = L_CACC + 15360;
constexpr u32 L_W = 0;
constexpr u32 L_ORED = 102400;
constexpr u32 L_LRED = L_ORED + 36864;
constexpr u32 L_A3 = L_QF;
constexpr u32 L_CNT = 163840 - 128;
constexpr u32 L_END = 163840;
static_assert(L_KV + 11520 + 10560 <= L_ORED && L_LRED + 2048 <= L_QF && L_PART + 8192 <= L_CNT, "lds");
enum { C_PART, C_KT, C_CS, C_HS, C_CD, C_WO, C_A3, C_AT0, C_AT1, C_OR, C_N };
#define CNT(i) (L_CNT + 4 * (i))

#define STAMP(i) do {} while (0)
#define CSTAMP(i) do {} while (0)
#define STAMP_INIT do {} while (0)
#define STAMP_OUT do {} while (0)

DI rsrc_t mkrs(const void* p, u32 bytes) { return __builtin_amdgcn_make_buffer_rsrc((void*)p, (short)0, (int)bytes, 0x00020000); }
DI u32 pk2(float a, float b) { f32x2 v = {a, b}; return __builtin_bit_cast(u32, __builtin_convertvector(v, bf16x2)); }
DI float lo2f(u32 x) { return __builtin_bit_cast(float, x << 16); }
DI float hi2f(u32 x) { return __builtin_bit_cast(float, x & 0xffff0000u); }
DI void lgkm0() { asm volatile("s_waitcnt lgkmcnt(0)" ::: "memory"); }
DI void vm0() { asm volatile("s_waitcnt vmcnt(0)" ::: "memory"); }
template <int N> DI void vmw() { asm volatile("s_waitcnt vmcnt(%0)" ::"n"(N) : "memory"); }
DI void bar() { asm volatile("s_waitcnt lgkmcnt(0)\n\ts_barrier" ::: "memory"); }
template <class T> DI LAS T* lds(u32 off) { return (LAS T*)(size_t)off; }
DI s16x4 trread(u32 addr) { return __builtin_bit_cast(s16x4, __builtin_amdgcn_ds_read_tr16_b64_v4i16((LAS s16x4*)(size_t)addr)); }
DI bf16x8 cat8(s16x4 a, s16x4 b) { return __builtin_bit_cast(bf16x8, __builtin_shufflevector(a, b, 0, 1, 2, 3, 4, 5, 6, 7)); }
DI void dma16(u32 lds_dst, u32 voff, rsrc_t rs, u32 soff) {
  u32 keep;
  asm volatile("s_mov_b32 %0, m0\n\ts_mov_b32 m0, %1\n\ts_nop 0\n\tbuffer_load_dwordx4 %2, %3, %4 offen lds\n\ts_mov_b32 m0, %0"
               : "=&s"(keep) : "s"(lds_dst), "v"(voff), "s"(rs), "s"(soff) : "memory");
}
DI void dma16nt(u32 lds_dst, u32 voff, rsrc_t rs, u32 soff) {
  u32 keep;
  asm volatile("s_mov_b32 %0, m0\n\ts_mov_b32 m0, %1\n\ts_nop 0\n\tbuffer_load_dwordx4 %2, %3, %4 offen nt lds\n\ts_mov_b32 m0, %0"
               : "=&s"(keep) : "s"(lds_dst), "v"(voff), "s"(rs), "s"(soff) : "memory");
}
DI u32 swap23(u32 x) { return (x & ~12u) | ((x & 4u) << 1) | ((x & 8u) >> 1); }
DI int mod5(int x) { return x >= 5 ? x - 5 : x; }

constexpr u32 SPIN_CAP = 1u << 17;
DI bool poll_flags(const char* p, bool active, u64 tag) {
  u64 v = tag;
  if (active) v = __hip_atomic_load((GAS u64*)p, __ATOMIC_RELAXED, __HIP_MEMORY_SCOPE_AGENT);
  return __all(v == tag);
}
template <int SLP = 1>
DI void wait_flags(const char* p, bool active, u64 tag) {
  if (__builtin_expect(!poll_flags(p, active, tag), 0)) {
#pragma unroll 1
    for (u32 spins = 0; spins < SPIN_CAP; ++spins) {
      __builtin_amdgcn_s_sleep(SLP);
      if (poll_flags(p, active, tag)) break;
    }
  }
  asm volatile("" ::: "memory");
}
DI void put_flag(char* p, u64 tag) { __hip_atomic_store((GAS u64*)p, tag, __ATOMIC_RELAXED, __HIP_MEMORY_SCOPE_AGENT); }
DI void lds_arrive(u32 cnt) {
  asm volatile("s_waitcnt lgkmcnt(0)\n\ts_mov_b64 exec, 1\n\tds_add_u32 %0, %1\n\ts_mov_b64 exec, -1" ::"v"(cnt), "v"(1u) : "memory");
}
template <int SLP = 0>
DI void lds_wait(u32 cnt, u32 target) {
  u32 v, sv, n = SPIN_CAP << 3;
  asm volatile("1:\n\tds_read_b32 %0, %3\n\ts_sub_u32 %2, %2, 1\n\ts_waitcnt lgkmcnt(0)\n\tv_readfirstlane_b32 %1, %0\n\t"
               "s_cselect_b32 %1, -1, %1\n\ts_cmp_lt_u32 %1, %4\n\ts_cbranch_scc0 2f\n\ts_sleep %5\n\ts_branch 1b\n2:"
               : "=&v"(v), "=&s"(sv), "+s"(n) : "v"(cnt), "s"(target), "n"(SLP) : "memory", "scc");
}

#define VSLEEP 8
#define CONV_PAD() do {} while (0)
template <int NA> struct CFrag { u32x4 a[NA], b[NA]; };
template <int NA> DI void conv_load(CFrag<NA>& f, u32 hb, u32 xb) {
#pragma unroll
  for (int a = 0; a < NA; ++a) {
    const u32x2 p = *lds<u32x2>(hb + 512 * a), q = *lds<u32x2>(hb + 512 * a + 8);
    f.a[a] = (u32x4){p[0], p[1], q[0], q[1]};
    f.b[NA - 1 - a] = *lds<u32x4>(xb + 512 * (8 - NA + a));
  }
  SB();
}
template <int MASK, int NA> DI void conv_mma(const CFrag<NA>& f, f32x4 (&acc)[8]) {
#pragma unroll
  for (int a = 0; a < NA; ++a)
#pragma unroll
    for (int c = NA - 1; c >= 0; --c)
      if (a + c < 8 && ((MASK >> (a + c)) & 1)) {
        acc[a + c] = __builtin_amdgcn_mfma_f32_16x16x32_bf16(__builtin_bit_cast(bf16x8, f.a[a]), __builtin_bit_cast(bf16x8, f.b[c]), acc[a + c], 0, 0, 0);
        CONV_PAD();
      }
  SB();
}
template <int MASK, int NA>
DI void conv_task(rsrc_t rsW, u32 img, u32 cacc, u32 yoff, int lane) {
  const int col = lane & 15, gq = lane >> 4;
  constexpr int NT_ = MASK == 0xF1 ? 5 : 3;
  u32 xb = img + 16640 + 480 - 32 * col + 16 * gq;
  const u32 e = col + 1 + 8 * gq;
  u32 hb = img + (e & 3) * 4160 + (e & ~3u) * 2;
  f32x4 acc[8];
#pragma unroll
  for (int i = 0; i < NT_; ++i) acc[MASK == 0xF1 ? (i < 4 ? 4 + i : 0) : 1 + i] = (f32x4){0.f, 0.f, 0.f, 0.f};
  CFrag<NA> f0, f1;
  conv_load(f0, hb, xb);
#pragma unroll 1
  for (int b = 0; b < 4; ++b) {
    conv_load(f1, hb + 64, xb + 64);
    conv_mma<MASK>(f0, acc);
    hb += 128;
    xb += 128;
    if (b < 3) conv_load(f0, hb, xb);
    conv_mma<MASK>(f1, acc);
  }
#pragma unroll
  for (int i = 0; i < NT_; ++i) {
    const f32x4 v = acc[MASK == 0xF1 ? (i < 4 ? 4 + i : 0) : 1 + i];
    *lds<u32x2>(cacc + 512 * i + 32 * col + 8 * gq) = (u32x2){pk2(v[0], v[1]), pk2(v[2], v[3])};
  }
  lgkm0();
  if (MASK == 0xF1) {
    const u32x4 s0 = *lds<u32x4>(cacc + lane * 16), s1 = *lds<u32x4>(cacc + 1024 + lane * 16), s2 = *lds<u32x4>(cacc + 2048 + (lane & 31) * 16);
    __builtin_amdgcn_raw_buffer_store_b128(s0, rsW, yoff + 2048 + lane * 16, 0, SC1);
    __builtin_amdgcn_raw_buffer_store_b128(s1, rsW, yoff + 3072 + lane * 16, 0, SC1);
    __builtin_amdgcn_raw_buffer_store_b128(s2, rsW, lane < 32 ? yoff + lane * 16 : OOB, 0, SC1);
  } else {
    const u32x4 s0 = *lds<u32x4>(cacc + lane * 16), s1 = *lds<u32x4>(cacc + 1024 + (lane & 31) * 16);
    __builtin_amdgcn_raw_buffer_store_b128(s0, rsW, yoff + 512 + lane * 16, 0, SC1);
    __builtin_amdgcn_raw_buffer_store_b128(s1, rsW, lane < 32 ? yoff + 1536 + lane * 16 : OOB, 0, SC1);
  }
}
struct Rec { u32x4 k1a, k1b, v0, v1, v2, v3; u32x2 k2a, k2b; };
#define LD128(off, so) __builtin_amdgcn_raw_buffer_load_b128(rsW, v16 + (off), so, SC1)
#define LD64(off, so) __builtin_amdgcn_raw_buffer_load_b64(rsW, v8 + (off), so, SC1)
#define LOAD_REC(R, so) do { \
    R.k1a = LD128(0, so); SB(); R.k2a = LD64(2048, so); SB(); R.v0 = LD128(3072, so); SB(); R.v1 = LD128(0, (so) + 4096); SB(); \
    R.k1b = LD128(1024, so); SB(); R.k2b = LD64(2560, so); SB(); R.v2 = LD128(1024, (so) + 4096); SB(); } while (0)
#define LOAD_FIRST() do { LOAD_REC(A, rec0); A.v3 = LD128(2048, rec0 + 4096); SB(); LOAD_REC(B, rec0 + 7168); } while (0)
DI f32x16 qk16(u32x4 k1, u32x4 q1) {
  f32x16 z;
#pragma unroll
  for (int i = 0; i < 16; ++i) z[i] = 0.f;
  return __builtin_amdgcn_mfma_f32_32x32x16_bf16(__builtin_bit_cast(bf16x8, k1), __builtin_bit_cast(bf16x8, q1), z, 0, 0, 0);
}
DI f32x16 qk8(u32x2 k2, u32x2 q2, f32x16 s) {
  return __builtin_amdgcn_mfma_f32_32x32x8bf16_1k(__builtin_bit_cast(s16x4, k2), __builtin_bit_cast(s16x4, q2), s, 0, 0, 0);
}
DI f32x16 pv_lo(u32x4 v, f32x16 p, f32x16 o, int idx) {
  f32x8 h = __builtin_shufflevector(p, p, 0, 1, 2, 3, 4, 5, 6, 7);
  return __builtin_amdgcn_smfmac_f32_32x32x32_bf16(__builtin_bit_cast(bf16x8, v), __builtin_bit_cast(bf16x16, h), o, idx, 0, 0);
}
DI f32x16 pv_hi(u32x4 v, f32x16 p, f32x16 o, int idx) {
  f32x8 h = __builtin_shufflevector(p, p, 8, 9, 10, 11, 12, 13, 14, 15);
  return __builtin_amdgcn_smfmac_f32_32x32x32_bf16(__builtin_bit_cast(bf16x8, v), __builtin_bit_cast(bf16x16, h), o, idx, 0, 0);
}
#define EXP4(X, i) do { X[i] = __builtin_amdgcn_exp2f(X[i]); X[i + 1] = __builtin_amdgcn_exp2f(X[i + 1]); X[i + 2] = __builtin_amdgcn_exp2f(X[i + 2]); X[i + 3] = __builtin_amdgcn_exp2f(X[i + 3]); } while (0)
#define STEP(H, VPREV, VCUR, K1N, K2N, REFILL) do { \
    EXP4(Sx[H], 0); SB(); \
    O[(H + 3) & 3] = pv_hi(VPREV, Sx[(H + 3) & 3], O[(H + 3) & 3], idx); SB(); \
    EXP4(Sx[H], 4); SB(); \
    Sx[(H + 2) & 3] = qk16(K1N, q1[(H + 2) & 3]); SB(); \
    EXP4(Sx[H], 8); SB(); \
    Sx[(H + 2) & 3] = qk8(K2N, q2[(H + 2) & 3], Sx[(H + 2) & 3]); SB(); \
    EXP4(Sx[H], 12); SB(); \
    O[H] = pv_lo(VCUR, Sx[H], O[H], idx); SB(); \
    REFILL; SB(); \
  } while (0)
#define REC_STEPS(R, N, so, sn) do { \
    STEP(0, N.v3, R.v0, R.k1a, R.k2a, N.v3 = LD128(2048, (sn) + 4096)); \
    STEP(1, R.v1, R.v0, R.k1a, R.k2a, R.k1a = LD128(0, so)); \
    STEP(2, R.v1, R.v0, R.k1b, R.k2b, R.k2a = LD64(2048, so)); \
    STEP(3, R.v1, R.v0, R.k1b, R.k2b, R.v0 = LD128(3072, so)); \
    STEP(0, R.v1, R.v2, R.k1b, R.k2b, R.v1 = LD128(0, (so) + 4096)); \
    STEP(1, R.v3, R.v2, R.k1b, R.k2b, R.k1b = LD128(1024, so)); \
    STEP(2, R.v3, R.v2, N.k1a, N.k2a, R.k2b = LD64(2560, so)); \
    STEP(3, R.v3, R.v2, N.k1a, N.k2a, R.v2 = LD128(1024, (so) + 4096)); \
  } while (0)

__global__ void __launch_bounds__(512, 2)
fused_kernel(const u16* __restrict__ Bx, const u16* __restrict__ Bwk, const u16* __restrict__ Bwv, const u16* __restrict__ Bwq, char* ws,
             const u16* __restrict__ Ak, const u16* __restrict__ Av, const u16* __restrict__ Ash, const u16* __restrict__ Assm,
             const u16* __restrict__ Aq, const u16* __restrict__ AD, const u16* __restrict__ Bwo, u16* __restrict__ out) {
  __shared__ __attribute__((aligned(16))) char smem[L_END];
  asm volatile("" ::"v"((u32)(size_t)(LAS char*)smem));
  const int lane = threadIdx.x & 63;
  const int w = __builtin_amdgcn_readfirstlane(threadIdx.x >> 6);
  const int bj = blockIdx.x >> 3, g = (blockIdx.x & 7) * 4 + (bj >> 3), hq = bj & 7;
  const int gq = lane >> 4, l15 = lane & 15;
  const int half = w >> 2, wq4 = w & 3, h = lane >> 5, l31 = lane & 31;
  const int qt = (hq >> 2) ^ half;
  STAMP_INIT;
  STAMP(0); CSTAMP(29);
  const rsrc_t rsW = mkrs(ws, WS_END), rsX = mkrs(Bx, S * D * 2), rsQ = mkrs(Bwq, D * D * 2);
  const rsrc_t rsK = mkrs((hq & 1) ? Bwv : Bwk, D * 20 * 2);
  asm volatile("s_nop 4" ::"s"(rsW), "s"(rsX), "s"(rsQ), "s"(rsK));

  u32x4 q1[4];
  u32x2 q2[4];
  u64 tag;
  Rec A, B;
  const u32 v16 = lane * 16, v8 = lane * 8, rec0 = WS_REC + wq4 * 8 * 7168;
  const char* kvflag = ws + WS_FKV + (blockIdx.x & (NREP - 1)) * 2048 + (wq4 * 64 + lane) * 8;
  {
    const int rot = g & 3;
    const int rbk = hq >> 1;
    const u32 xvo = gq * 1280 + ((l15 ^ (4 * gq)) * 16);
    if (w == 0) {
      if (lane < C_N) *lds<u32>(CNT(lane)) = 0;
      lgkm0();
    }
    u64 ep;
    asm volatile("s_load_dwordx2 %0, %1, 0x0" : "=s"(ep) : "s"(ws + WS_E) : "memory");
    u32 pfs[3] = {0, 0, 0};
    if (w >= 3) {
      const int j = w - 3;
#pragma unroll
      for (int m = 0; m < 4; ++m) dma16(L_X + (j * 16 + 4 * rbk + m) * 1056, xvo, rsX, (64 * g + 16 * rbk + 4 * m) * 1280 + 256 * j);
      const int i0 = (bj >> 1) % 5;
#pragma unroll
      for (int i = 0; i < 5; ++i) { const u32 o = j * 5120 + mod5(i0 + i) * 1024; dma16(L_WK + o, lane * 16, rsK, o); }
    }
    else {
      u64 pc;
      u32 d0, d1, d2, len;
      asm volatile("s_getpc_b64 %0\n.Lpf_pc%=:\n\ts_mov_b32 %1, .Lpf_end-.Lpf_pc%=" : "=s"(pc), "=s"(len));
      pc += min((u32)(bj * 9 + w * 3) * 128, len - 0x104);
      asm volatile("s_load_dword %0, %3, 0x0\n\ts_load_dword %1, %3, 0x80\n\ts_load_dword %2, %3, 0x100" : "=s"(d0), "=s"(d1), "=s"(d2) : "s"(pc) : "memory");
      pfs[0] = d0; pfs[1] = d1; pfs[2] = d2;
    }
    auto wsrc = [&](int ph) {
      const u32 c = 64 * ph + lane, p = c / 10;
      return swap23(p) * 1280 + (c - p * 10) * 16;
    };
    auto dma_x = [&](int s, int q) {
      const int j = mod5(rot + s), m = (4 * rbk + 4 + 4 * q) & 15;
#pragma unroll
      for (int i = 0; i < 4; ++i) dma16(L_X + (j * 16 + m + i) * 1056, xvo, rsX, (64 * g + 4 * (m + i)) * 1280 + 256 * j);
    };
    const bool dw = w < 5;
    const rsrc_t rsD = mkrs(dw ? Bwq : Bx, dw ? D * D * 2 : S * D * 2);
    const u32 dvo = dw ? wsrc(w) : xvo, dls = dw ? 5120 : 1056, dss = dw ? 40960 : 5120;
    const u32 m0 = (4 * rbk + 4 * (w - 4)) & 15;
    u32 dl, ds;
    auto duty = [&](int b, int i) {
      if (i == 0) {
        const u32 jx = mod5(rot + b + 1);
        dl = dw ? L_RING + (b % 3) * 20480 + w * 1024 : L_X + (jx * 16 + m0) * 1056;
        ds = dw ? mod5(rot + b) * 128 * 1280 + hq * 160 : (64 * g + 4 * m0) * 1280 + 256 * jx;
      } else {
        asm volatile("s_add_u32 %0, %0, %2\n\ts_add_u32 %1, %1, %3" : "+s"(dl), "+s"(ds) : "s"(dls), "s"(dss) : "scc");
      }
      dma16(dl, dvo, rsD, ds);
    };
    STAMP(13);
    asm volatile("s_barrier" ::: "memory");
    if (w < 3) {
      const u32 wv2 = wsrc(w + 3);
#pragma unroll
      for (int i = 0; i < 4; ++i) duty(0, i);
      if (w < 2) {
#pragma unroll
        for (int i = 0; i < 4; ++i) dma16(L_RING + i * 5120 + (w + 3) * 1024, wv2, rsQ, (rot * 128 + i * 32) * 1280 + hq * 160);
      }
      dma_x(0, w);
      dma_x(1, w);
      STAMP(14);
    } else {
      asm volatile("s_waitcnt vmcnt(0) lgkmcnt(0)" ::: "memory");
      STAMP(14);
      const int j = w - 3;
      const u32 rb = L_WK + j * 5120 + (8 * gq + ((lane >> 2) & 3)) * 40 + 8 * (lane & 3);
      const u32 xr = L_X + (j * 16 + 4 * rbk + (l15 >> 2)) * 1056 + (l15 & 3) * 256 + gq * 16;
      f32x4 a0 = {0.f, 0.f, 0.f, 0.f}, a1v = a0;
      bf16x8 w0[4], w1[4], xk[4];
#pragma unroll
      for (int ks = 0; ks < 4; ++ks) {
        xk[ks] = *lds<bf16x8>(xr + ((ks ^ (l15 & 3)) * 64));
        w0[ks] = cat8(trread(rb + ks * 1280), trread(rb + ks * 1280 + 160));
        w1[ks] = cat8(trread(rb + ks * 1280 + 32), trread(rb + ks * 1280 + 192));
      }
      SB();
      if (hq & 1) {
#pragma unroll
        for (int ks = 0; ks < 4; ++ks) {
          a0 = __builtin_amdgcn_mfma_f32_16x16x32_bf16(xk[ks], w0[ks], a0, 0, 0, 0);
          a1v = __builtin_amdgcn_mfma_f32_16x16x32_bf16(xk[ks], w1[ks], a1v, 0, 0, 0);
        }
      } else {
#pragma unroll
        for (int ks = 0; ks < 4; ++ks) {
          a0 = __builtin_amdgcn_mfma_f32_16x16x32_bf16(w0[ks], xk[ks], a0, 0, 0, 0);
          a1v = __builtin_amdgcn_mfma_f32_16x16x32_bf16(w1[ks], xk[ks], a1v, 0, 0, 0);
        }
      }
      if (w < 7) {
        *lds<f32x4>(L_PART + j * 2048 + lane * 16) = a0;
        *lds<f32x4>(L_PART + j * 2048 + 1024 + lane * 16) = a1v;
        lds_arrive(CNT(C_PART));
      } else {
        __builtin_amdgcn_s_setprio(3);
        lds_wait(CNT(C_PART), 4);
        f32x4 p0[4], p1[4];
#pragma unroll
        for (int i = 0; i < 4; ++i) {
          p0[i] = *lds<f32x4>(L_PART + i * 2048 + lane * 16);
          p1[i] = *lds<f32x4>(L_PART + i * 2048 + 1024 + lane * 16);
        }
        SB();
        a0 += (p0[0] + p0[1]) + (p0[2] + p0[3]);
        a1v += (p1[0] + p1[1]) + (p1[2] + p1[3]);
        u32x2 o0 = {pk2(a0[0], a0[1]), pk2(a0[2], a0[3])}, o1 = {pk2(a1v[0], a1v[1]), pk2(a1v[2], a1v[3])};
        const u32 rec = WS_REC + g * 7168;
        if (hq & 1) {
          if (l15 == 4) o1 = (u32x2){0x3f803f80u, 0x3f803f80u};
          if (l15 > 4) o1 = (u32x2){0u, 0u};
          const auto x = __builtin_amdgcn_permlane16_swap(o0[0], o1[0], false, false);
          const auto y = __builtin_amdgcn_permlane16_swap(o0[1], o1[1], false, false);
          const u32 rho = (gq & 1) ? 16 + l15 : swap23(l15);
          __builtin_amdgcn_raw_buffer_store_b128((u32x4){x[0], y[0], x[1], y[1]}, rsW, rec + 3072 + rbk * 1024 + (gq >> 1) * 512 + rho * 16, 0, SC1);
        } else {
          const float c = 0.22360679774997896f * 1.4426950408889634f;
          o0 = (u32x2){pk2(lo2f(o0[0]) * c, hi2f(o0[0]) * c), pk2(lo2f(o0[1]) * c, hi2f(o0[1]) * c)};
          o1 = (u32x2){pk2(lo2f(o1[0]) * c, hi2f(o1[0]) * c), pk2(lo2f(o1[1]) * c, hi2f(o1[1]) * c)};
          if (gq == 1) o1 = (u32x2){0x3f803f80u, 0x00003f80u};
          const auto x = __builtin_amdgcn_permlane32_swap(o0[0], o0[0], false, false);
          const auto y = __builtin_amdgcn_permlane32_swap(o0[1], o0[1], false, false);
          const u32 T = rbk >> 1, key = 16 * (rbk & 1) + l15 + 32 * gq;
          __builtin_amdgcn_raw_buffer_store_b128((u32x4){x[0], y[0], x[1], y[1]}, rsW, gq < 2 ? rec + T * 1024 + key * 16 : OOB, 0, SC1);
          __builtin_amdgcn_raw_buffer_store_b64(o1, rsW, gq < 2 ? rec + 2048 + T * 512 + key * 8 : OOB, 0, SC1);
        }
        STAMP(17);
      }
    }
#pragma unroll
    for (int i = 0; i < 4; ++i) duty(1, i);
    asm volatile("s_waitcnt lgkmcnt(0)" : "+s"(ep) : "s"(pfs[0]), "s"(pfs[1]), "s"(pfs[2]) : "memory");
    tag = ep + 0x9E3779B97F4A7C15ull;
    f32x4 acc[3];
    u32 qa[3];
#pragma unroll
    for (int i = 0; i < 3; ++i) {
      acc[i] = (f32x4){0.f, 0.f, 0.f, 0.f};
      const u32 G = 4 * (2 * half + i) + gq, hd = G / 5, ga = G - 5 * hd, q = 16 * (wq4 & 1) + l15;
      qa[i] = L_QF + ((wq4 >> 1) ^ (hq >> 2)) * 6144 + (ga < 4 ? hd * 1024 + (q + 32 * (ga & 1)) * 16 + (ga >> 1) * 8 : 4096 + hd * 512 + q * 8);
    }
    const u32 wl = L_RING + (16 * (gq >> 1) + 4 * (gq & 1) + ((lane >> 2) & 3)) * 160 + half * 64 + 8 * (lane & 3);
    u32 xa[4];
#pragma unroll
    for (int i = 0; i < 4; ++i) xa[i] = L_X + (4 * wq4 + (l15 >> 2)) * 1056 + (l15 & 3) * 256 + gq * 16 + ((i ^ (l15 & 3)) * 64);
    bf16x8 xf[2][4];
    u64 fl = 0;
#pragma unroll
    for (int s = 0; s < 5; ++s) {
      if (s < 3 || (s == 3 && dw)) vmw<4>(); else vm0();
      if (s == 0) STAMP(15);
      bar();
      if (s == 0) {
        if (w == 7) {
          if (lane < NREP) put_flag(ws + WS_FKV + lane * 2048 + (g * 8 + hq) * 8, tag);
          __builtin_amdgcn_s_setprio(0);
        }
        STAMP(16);
      }
      if (s == 1) STAMP(18);
      if (s == 4) STAMP(19);
      if (s == 4 && half == 0) fl = __hip_atomic_load((GAS u64*)kvflag, __ATOMIC_RELAXED, __HIP_MEMORY_SCOPE_AGENT);
      bf16x8 wf[4][3];
      auto rdw = [&](int ks) {
        if (s == 0) xf[0][ks] = *lds<bf16x8>(xa[ks] + rot * 16896);
#pragma unroll
        for (int i = 0; i < 3; ++i) {
          const u32 wb = wl + (s % 3) * 20480 + ks * 5120 + i * 32;
          wf[ks][i] = cat8(trread(wb), trread(wb + 1280));
        }
      };
      rdw(0);
      rdw(1);
      SB();
      const u32 xo = mod5(rot + s + 1) * 16896;
#pragma unroll
      for (int ks = 0; ks < 4; ++ks) {
#pragma unroll
        for (int i = 0; i < 3; ++i) acc[i] = __builtin_amdgcn_mfma_f32_16x16x32_bf16(wf[ks][i], xf[s & 1][ks], acc[i], 0, 0, 0);
        SB();
        if (ks < 2) rdw(ks + 2);
        else if (s < 4) {
          xf[(s + 1) & 1][2 * ks - 4] = *lds<bf16x8>(xa[2 * ks - 4] + xo);
          xf[(s + 1) & 1][2 * ks - 3] = *lds<bf16x8>(xa[2 * ks - 3] + xo);
        }
        if (s < 2) duty(s + 2, ks);
        if (s == 2 && ks == 1 && dw) {
#pragma unroll
          for (int i = 0; i < 4; ++i) duty(4, i);
        }
        SB();
      }
    }
    STAMP(3);
#pragma unroll
    for (int i = 0; i < 3; ++i) *lds<u32x2>(qa[i]) = (u32x2){pk2(acc[i][0], acc[i][1]), pk2(acc[i][2], acc[i][3])};
    const u32 qf = L_QF + half * 6144;
    if (half == 0) {
      if (!__all(fl == tag)) wait_flags(kvflag, true, tag);
      asm volatile("" ::: "memory");
      STAMP(7); CSTAMP(30);
      LOAD_FIRST();
    }
    bar();
#pragma unroll
    for (int hd = 0; hd < 4; ++hd) {
      q1[hd] = *lds<u32x4>(qf + hd * 1024 + lane * 16);
      q2[hd] = *lds<u32x2>(qf + 4096 + hd * 512 + l31 * 8);
      if (h) q2[hd] = (u32x2){0u, 0u};
    }
  }
  STAMP(4);

  const int nch = g < 26 ? 3 : (g == 26 ? 2 : 0), ch0 = hq * 80 + 3 * g;
  if (__builtin_expect(half == 1, 0)) {
    __builtin_amdgcn_s_setprio(3);
    {
      const rsrc_t rsAk = mkrs(Ak, S * D * 2), rsAv = mkrs(Av, S * D * 2);
      const u32 r = lane / 10, vo = r * 1280 + (lane - r * 10) * 16 + hq * 160;
      const int rk = 64 * g - 3 + 18 * wq4;
      if (lane < 60) {
        dma16nt(L_KV + wq4 * 2880, rk < 0 ? (r < 3 ? OOB : vo - 3840) : vo, rsAk, rk < 0 ? 0 : rk * 1280);
        dma16nt(L_KV + wq4 * 2880 + 960, vo, rsAk, (rk + 6) * 1280);
        dma16nt(L_KV + wq4 * 2880 + 1920, (wq4 == 3 && r > 0) ? OOB : vo, rsAk, (rk + 12) * 1280);
        __builtin_amdgcn_s_sleep(VSLEEP);
        dma16nt(L_KV + 11520 + wq4 * 2880, vo, rsAv, (rk + 3) * 1280);
        dma16nt(L_KV + 11520 + wq4 * 2880 + 960, (wq4 == 3 && r > 3) ? OOB : vo, rsAv, (rk + 9) * 1280);
        if (wq4 < 3) dma16nt(L_KV + 11520 + wq4 * 2880 + 1920, vo, rsAv, (rk + 15) * 1280);
      }
    }
    if (wq4 < 3) {
      const int run = lane >> 3, cq = wq4 * 8 + (lane & 7);
      const bool a1 = cq < 20;
      u32x2 kr[11], vr[8];
      const rsrc_t rsSh = mkrs(Ash, D * 4 * 2);
      const u32 cb = hq * 160 + cq * 8;
      const u32x4 sw0 = __builtin_amdgcn_raw_buffer_load_b128(rsSh, a1 ? cb * 4 : OOB, 0, 0);
      const u32x4 sw1 = __builtin_amdgcn_raw_buffer_load_b128(rsSh, a1 ? cb * 4 + 16 : OOB, 0, 0);
      STAMP(40);
      vm0();
      lds_arrive(CNT(C_KT));
      lds_wait<1>(CNT(C_KT), 4);
      STAMP(41);
      const u32 ka = L_KV + run * 1280 + cq * 8;
#pragma unroll
      for (int i = 0; i < 11; ++i) kr[i] = *lds<u32x2>(ka + i * 160);
#pragma unroll
      for (int i = 0; i < 8; ++i) vr[i] = *lds<u32x2>(ka + 11520 + i * 160);
      SB();
      const u32 scr = L_A1 + wq4 * 4096;
#pragma unroll
      for (int c = 0; c < 4; ++c) {
        const u32 wa = (c < 2) ? sw0[2 * (c & 1)] : sw1[2 * (c & 1)], wb = (c < 2) ? sw0[2 * (c & 1) + 1] : sw1[2 * (c & 1) + 1];
        const float f0 = lo2f(wa), f1 = hi2f(wa), f2 = lo2f(wb), f3 = hi2f(wb);
        float kf[11], r[8];
#pragma unroll
        for (int i = 0; i < 11; ++i) kf[i] = (c & 1) ? hi2f(kr[i][c >> 1]) : lo2f(kr[i][c >> 1]);
#pragma unroll
        for (int i = 0; i < 8; ++i) {
          const float ks = fmaf(kf[i + 3], f0, fmaf(kf[i + 2], f1, fmaf(kf[i + 1], f2, kf[i] * f3)));
          r[i] = ks * ((c & 1) ? hi2f(vr[i][c >> 1]) : lo2f(vr[i][c >> 1]));
        }
        *lds<u32x4>(scr + ((lane & 7) * 4 + c) * 128 + ((7 - run) ^ (lane & 7)) * 16) =
            (u32x4){pk2(r[7], r[6]), pk2(r[5], r[4]), pk2(r[3], r[2]), pk2(r[1], r[0])};
      }
      lgkm0();
      STAMP(42);
      const u32 so = WS_KVT + (hq * 80 + wq4 * 32 + (lane >> 3)) * 4096 + (1984 - 64 * g) * 2 + (lane & 7) * 16;
#pragma unroll
      for (int i = 0; i < 4; ++i) {
        const u32 chl = 8 * i + (lane >> 3);
        if (wq4 < 2 || i < 2) {
          const u32x4 v = *lds<u32x4>(scr + chl * 128 + ((lane & 7) ^ (chl >> 2)) * 16);
          __builtin_amdgcn_raw_buffer_store_b128(v, rsW, so + i * 32768, 0, SC1);
        }
      }
      vm0();
      if (lane == 0) put_flag(ws + WS_FA1 + ((hq * 4 + wq4) * 32 + g) * 8, tag);
    }
    else {
      vm0();
      lds_arrive(CNT(C_KT));
      STAMP(40);
      const rsrc_t rsH = mkrs(Assm, D * S * 2);
      u32x4 t[3][4];
#pragma unroll
      for (int c = 0; c < 3; ++c)
#pragma unroll
        for (int i = 0; i < 4; ++i) t[c][i] = __builtin_amdgcn_raw_buffer_load_b128(rsH, lane * 16 + i * 1024, c < nch ? (ch0 + c) * 4096 : OOB, 0);
      SB();
      const u32x4 z4 = {0u, 0u, 0u, 0u};
#pragma unroll
      for (int c = 0; c < 3; ++c)
        if (lane < 40) *lds<u32x4>(L_CV + c * CV_CH + (lane < 8 ? (lane >> 1) * 4160 + (lane & 1) * 16 : 16640 + 4096 + (lane - 8) * 16)) = z4;
      lgkm0();
#pragma unroll
      for (int odd = 0; odd < 2; ++odd) {
#pragma unroll
        for (int c = 0; c < 3; ++c) {
          const u32 a = L_CV + c * CV_CH + odd * 4160 + lane * 16;
          if (odd == 0 && lane == 0) {
            *lds<u16>(a + 4160 + 30) = (u16)t[c][0][0];
            *lds<u16>(a + 3 * 4160 + 26) = (u16)t[c][0][0];
          }
#pragma unroll
          for (int i = 0; i < 4; ++i) {
            *lds<u32x4>(a + 32 + i * 1024) = t[c][i];
            asm volatile("ds_write2_b32 %0, %1, %2 offset0:7 offset1:8\n\tds_write2_b32 %0, %3, %4 offset0:9 offset1:10"
                         ::"v"(a + 8320 + i * 1024), "v"(t[c][i][0]), "v"(t[c][i][1]), "v"(t[c][i][2]), "v"(t[c][i][3]) : "memory");
          }
          if (odd == 0) {
            lgkm0();
#pragma unroll
            for (int i = 0; i < 4; ++i) t[c][i] = __builtin_amdgcn_raw_buffer_load_b128(rsH, lane * 16 + 2 + i * 1024, c < nch ? (ch0 + c) * 4096 : OOB, 0);
            SB();
          }
        }
        if (odd == 0) STAMP(41);
      }
      lds_arrive(CNT(C_HS));
      STAMP(42);
    }
    STAMP(2);
    if (wq4 < nch) {
      const int ch = ch0 + wq4;
      const u32 img = L_CV + wq4 * CV_CH;
      u32x4 t[4];
      wait_flags<2>(ws + WS_FA1 + ((hq * 4 + ((3 * g + wq4) >> 5)) * 32 + l31) * 8, true, tag);
      STAMP(43);
#pragma unroll
      for (int i = 0; i < 4; ++i) t[i] = __builtin_amdgcn_raw_buffer_load_b128(rsW, lane * 16 + i * 1024, WS_KVT + ch * 4096, SC1);
#pragma unroll
      for (int i = 0; i < 4; ++i) *lds<u32x4>(img + 16640 + lane * 16 + i * 1024) = t[i];
      lds_arrive(CNT(C_CS));
      lds_wait<2>(CNT(C_HS), 1);
      STAMP(5);
      conv_task<0xF1, 8>(rsW, img, L_CACC + wq4 * 4096, WS_YT + ch * 4096, lane);
    } else if (wq4 == 3) {
      lds_wait<4>(CNT(C_CS), nch);
      STAMP(5);
#pragma unroll 1
      for (int i = 0; i < nch; ++i) conv_task<0x0E, 4>(rsW, L_CV + i * CV_CH, L_CACC + 12288, WS_YT + (ch0 + i) * 4096, lane);
    }
    lds_arrive(CNT(C_CD));
    vm0();
    if (lane == 0 && nch) put_flag(ws + WS_FY + ((hq * 32 + g) * 4 + wq4) * 8, tag);
    STAMP(6);
    STAMP(7); CSTAMP(30);
    LOAD_FIRST();
    __builtin_amdgcn_s_setprio(1);
  } else {
    __builtin_amdgcn_s_setprio(2);
  }
  {
    const int idx = (int)0xDDDDDDDDu;
    const u32 cntA = half ? CNT(C_AT1) : CNT(C_AT0);
    u32 round = 0;
    f32x16 O[4];
    float lsum;
#pragma unroll 1
    for (int attempt = 0; attempt < 2; ++attempt) {
      if (__builtin_expect(attempt == 1, 0)) {
        float m[4] = {-INFINITY, -INFINITY, -INFINITY, -INFINITY};
#pragma unroll 1
        for (int r = 0; r < 16; ++r) {
          const u32 so = rec0 + (r >> 1) * 7168;
          const u32x4 k1 = LD128(0, so + (r & 1) * 1024);
          const u32x2 k2 = LD64(2048, so + (r & 1) * 512);
#pragma unroll
          for (int hd = 0; hd < 4; ++hd) {
            const f32x16 sa = qk8(k2, q2[hd], qk16(k1, q1[hd]));
#pragma unroll
            for (int i = 0; i < 16; ++i) m[hd] = fmaxf(m[hd], sa[i]);
          }
        }
#pragma unroll
        for (int hd = 0; hd < 4; ++hd) *lds<float>(L_ORED + (((l31 * 4 + hd) * 4 + wq4) * 2 + h) * 4) = m[hd];
        lds_arrive(cntA);
        lds_wait(cntA, 4 * (++round));
#pragma unroll
        for (int hd = 0; hd < 4; ++hd) {
          const f32x4 a = *lds<f32x4>(L_ORED + (l31 * 4 + hd) * 32), b = *lds<f32x4>(L_ORED + (l31 * 4 + hd) * 32 + 16);
          const float M = -fmaxf(fmaxf(fmaxf(a[0], a[1]), fmaxf(a[2], a[3])), fmaxf(fmaxf(b[0], b[1]), fmaxf(b[2], b[3])));
          const u32 m0 = __builtin_bit_cast(u32, M) & 0xffff0000u;
          const float r1 = M - __builtin_bit_cast(float, m0);
          const u32 m1 = __builtin_bit_cast(u32, r1) & 0xffff0000u;
          const float r2 = r1 - __builtin_bit_cast(float, m1);
          if (h) q2[hd] = (u32x2){(m0 >> 16) | m1, __builtin_bit_cast(u32, r2) >> 16};
        }
        lds_arrive(cntA);
        lds_wait(cntA, 4 * (++round));
        LOAD_FIRST();
      }
      f32x16 Sx[4];
#pragma unroll
      for (int i = 0; i < 4; ++i)
#pragma unroll
        for (int j = 0; j < 16; ++j) { O[i][j] = 0.f; Sx[i][j] = 0.f; }
      B.v3 = (u32x4){0u, 0u, 0u, 0u};
      Sx[0] = qk8(A.k2a, q2[0], qk16(A.k1a, q1[0]));
      Sx[1] = qk8(A.k2a, q2[1], qk16(A.k1a, q1[1]));
      STAMP(28);
#pragma unroll 1
      for (int r = 0; r < 8; r += 2) {
        const u32 s1 = rec0 + (r + 1) * 7168, s2 = r < 6 ? s1 + 7168 : OOB, s3 = r < 6 ? s1 + 14336 : OOB;
        SB();
        REC_STEPS(A, B, s2, s1);
        STAMP(32 + r);
        REC_STEPS(B, A, s3, s2);
        STAMP(33 + r);
      }
      O[3] = pv_hi(B.v3, Sx[3], O[3], idx);
      STAMP(8); CSTAMP(31);
      if (half == 1 && attempt == 0) lds_wait(CNT(C_OR), 4);
#pragma unroll
      for (int hd = 0; hd < 4; ++hd) {
        if (hd != wq4) {
          const u32 slot = hd * 3 + (wq4 > hd ? wq4 - 1 : wq4);
#pragma unroll
          for (int i = 0; i < 3; ++i)
            *lds<f32x4>(L_ORED + (slot * 3 + i) * 1024 + lane * 16) = (f32x4){O[hd][4 * i], O[hd][4 * i + 1], O[hd][4 * i + 2], O[hd][4 * i + 3]};
        }
        if (h) *lds<float>(L_LRED + ((l31 * 4 + hd) * 4 + wq4) * 4) = O[hd][8];
      }
      STAMP(20);
      lds_arrive(cntA);
      lds_wait(cntA, 4 * (++round));
      STAMP(21);
      const f32x4 la = *lds<f32x4>(L_LRED + (l31 * 4 + 2 * h) * 16), lb = *lds<f32x4>(L_LRED + (l31 * 4 + 2 * h + 1) * 16);
      const f32x4 lo = *lds<f32x4>(L_LRED + (l31 * 4 + wq4) * 16);
      const float sa = (la[0] + la[1]) + (la[2] + la[3]), sb = (lb[0] + lb[1]) + (lb[2] + lb[3]);
      lsum = (lo[0] + lo[1]) + (lo[2] + lo[3]);
      const float LO = 5.421010862427522e-20f, HI = 1.8446744073709552e19f;
      const bool ok = sa >= LO && sa <= HI && sb >= LO && sb <= HI;
      if (__builtin_expect(__all(ok) || attempt == 1, 1)) break;
      lds_arrive(cntA);
      lds_wait(cntA, 4 * (++round));
    }
    f32x4 t[3];
#pragma unroll
    for (int hd = 0; hd < 4; ++hd)
      if (hd == wq4) {
#pragma unroll
        for (int i = 0; i < 3; ++i) t[i] = (f32x4){O[hd][4 * i], O[hd][4 * i + 1], O[hd][4 * i + 2], O[hd][4 * i + 3]};
      }
    f32x4 pr[9];
#pragma unroll
    for (int i = 0; i < 9; ++i) pr[i] = *lds<f32x4>(L_ORED + (wq4 * 9 + i) * 1024 + lane * 16);
    SB();
#pragma unroll
    for (int i = 0; i < 3; ++i) t[i] += pr[i] + (pr[3 + i] + pr[6 + i]);
    if (half == 0) lds_arrive(CNT(C_OR));
    const float inv = __builtin_amdgcn_rcpf(lsum);
    const u32x4 o0 = {pk2(t[0][0] * inv, t[0][1] * inv), pk2(t[0][2] * inv, t[0][3] * inv), pk2(t[1][0] * inv, t[1][1] * inv), pk2(t[1][2] * inv, t[1][3] * inv)};
    const u32x2 o1 = {pk2(t[2][0] * inv, t[2][1] * inv), pk2(t[2][2] * inv, t[2][3] * inv)};
    const u32 u = 4 * (hq & 1) + wq4;
    const u32 oo = WS_O + ((4 * g + 2 * qt + (l31 >> 4)) * 20 + 5 * (hq >> 1)) * 1024 + l15 * 16;
    __builtin_amdgcn_raw_buffer_store_b128(o0, rsW, oo + (u >> 1) * 1024 + (2 * (u & 1) + h) * 256, 0, SC1);
    __builtin_amdgcn_raw_buffer_store_b64(o1, rsW, h ? OOB : oo + 4096 + (u >> 1) * 256 + (u & 1) * 8, 0, SC1);
    STAMP(22);
    vm0();
    if (lane == 0) put_flag(ws + WS_FO + (((g * 2 + qt) * 8 + hq) * 4 + wq4) * 8, tag);
    STAMP(9);
  }

  if (half == 0) {
    if (blockIdx.x == 0 && threadIdx.x == 0) put_flag(ws + WS_E, tag);
    lds_wait<2>(CNT(C_CD), 4);
    STAMP(23);
    {
      const rsrc_t rsO = mkrs(Bwo, D * D * 2);
#pragma unroll
      for (int ph = 0; ph < 5; ++ph) {
        const u32 c = 64 * ph + lane, pos = c / 10, j = c - pos * 10, kp = swap23(pos);
        const u32 vm = (kp + (kp >> 4) * 4) * 1280 + j * 16, vt = ((kp >> 2) * 20 + 16 + (kp & 3)) * 1280 + j * 16;
#pragma unroll
        for (int blk = 0; blk < 4; ++blk) {
          const int b = (blk + g) & 3;
          dma16(L_W + wq4 * 25600 + b * 5120 + ph * 1024, vm, rsO, (wq4 * 160 + b * 40) * 1280 + hq * 160);
          vmw<2>();
        }
        dma16(L_W + wq4 * 25600 + 20480 + ph * 1024, vt, rsO, wq4 * 160 * 1280 + hq * 160);
        vmw<2>();
      }
      vm0();
      lds_arrive(CNT(C_WO));
    }
    __builtin_amdgcn_s_setprio(0);
    STAMP(24);
    const rsrc_t rsOut = mkrs(out, S * 2 * D * 2);
    if (w < 3) {
      const rsrc_t rsAq = mkrs(Aq, S * D * 2), rsD = mkrs(AD, D * 2);
      const int run = lane >> 3, cq = w * 8 + (lane & 7);
      const bool a3 = cq < 20;
      const u32 t0 = 64 * g + 8 * run, cb = hq * 160 + cq * 8;
      u32x4 yv[4], kvv[4];
      u32 dd[4];
      u32x2 qr[8];
#pragma unroll
      for (int i = 0; i < 8; ++i) qr[i] = __builtin_amdgcn_raw_buffer_load_b64(rsAq, a3 ? t0 * 1280 + cb : OOB, i * 1280, 0);
      const int ti = g >> 2;
      const bool small = ti >= 1 && ti <= 3;
      {
        const int p0 = lane, p1 = lane + 64;
        wait_flags(ws + WS_FY + (hq * 128 + p0) * 8, small ? (p0 & 3) == 3 : (p0 & 3) != 3, tag);
        wait_flags(ws + WS_FY + (hq * 128 + p1) * 8, p1 < 108 && (small ? (p1 & 3) == 3 : (p1 & 3) != 3), tag);
      }
#pragma unroll
      for (int i = 0; i < 4; ++i) {
        const u32 ch = hq * 80 + w * 32 + 8 * i + (lane >> 3);
        const bool on = w < 2 || i < 2;
        yv[i] = __builtin_amdgcn_raw_buffer_load_b128(rsW, on ? ch * 4096 + 128 * g + (lane & 7) * 16 : OOB, WS_YT, SC1);
        kvv[i] = __builtin_amdgcn_raw_buffer_load_b128(rsW, on ? ch * 4096 + 4080 - 128 * g - (lane & 7) * 16 : OOB, WS_KVT, SC1);
        dd[i] = __builtin_amdgcn_raw_buffer_load_b16(rsD, on ? ch * 2 : OOB, 0, 0);
      }
      SB();
      const u32 scr = L_A3 + w * 4096;
#pragma unroll
      for (int i = 0; i < 4; ++i) {
        const float dc = lo2f(dd[i]);
        u32x4 y2;
#pragma unroll
        for (int j = 0; j < 4; ++j) {
          const u32 kp = kvv[i][3 - j];
          const u32 m = pk2(hi2f(kp) * dc, lo2f(kp) * dc);
          y2[j] = pk2(lo2f(yv[i][j]) + lo2f(m), hi2f(yv[i][j]) + hi2f(m));
        }
        const u32 chl = 8 * i + (lane >> 3);
        *lds<u32x4>(scr + chl * 128 + ((lane & 7) ^ (chl >> 2)) * 16) = y2;
      }
      lgkm0();
      u32x4 z[4];
#pragma unroll
      for (int c = 0; c < 4; ++c) z[c] = *lds<u32x4>(scr + ((lane & 7) * 4 + c) * 128 + (run ^ (lane & 7)) * 16);
#pragma unroll
      for (int i = 0; i < 8; ++i) {
        float r[4];
#pragma unroll
        for (int c = 0; c < 4; ++c)
          r[c] = ((i & 1) ? hi2f(z[c][i >> 1]) : lo2f(z[c][i >> 1])) * ((c & 1) ? hi2f(qr[i][c >> 1]) : lo2f(qr[i][c >> 1]));
        __builtin_amdgcn_raw_buffer_store_b64((u32x2){pk2(r[0], r[1]), pk2(r[2], r[3])}, rsOut, a3 ? (t0 + i) * 2560 + cb : OOB, 0, 0);
      }
    }
    STAMP(10);
    lds_wait(CNT(C_WO), 4);
    f32x4 acc[5];
#pragma unroll
    for (int i = 0; i < 5; ++i) acc[i] = (f32x4){0.f, 0.f, 0.f, 0.f};
    const u32 wl = L_W + (16 * (gq >> 1) + 4 * (gq & 1) + ((lane >> 2) & 3)) * 160 + 8 * (lane & 3);
#pragma unroll 1
    for (int st = 0; st < 2; ++st) {
      const int kh = (w >> 1) ^ st;
      const u32 wb = wl + kh * 51200;
      bf16x8 wf[6][5];
#pragma unroll
      for (int ks = 0; ks < 6; ++ks)
#pragma unroll
        for (int nt = 0; nt < 5; ++nt) wf[ks][nt] = cat8(trread(wb + ks * 5120 + nt * 32), trread(wb + ks * 5120 + 1280 + nt * 32));
      wait_flags(ws + WS_FO + (((g * 2 + (w >> 1)) * 8 + 4 * kh) * 4 + l15) * 8, true, tag);
      if (st == 1) STAMP(11); else STAMP(25);
      u32x4 xf[10];
#pragma unroll
      for (int ks = 0; ks < 10; ++ks)
        xf[ks] = __builtin_amdgcn_raw_buffer_load_b128(rsW, lane * 16 + (ks & 3) * 1024, WS_O + ((4 * g + w) * 20 + 10 * kh + (ks & ~3)) * 1024, SC1);
      SB();
#pragma unroll
      for (int ks = 0; ks < 10; ++ks)
#pragma unroll
        for (int nt = 0; nt < 5; ++nt) {
          const bf16x8 wv = ks < 6 ? wf[ks < 6 ? ks : 0][nt] : cat8(trread(wb + ks * 5120 + nt * 32), trread(wb + ks * 5120 + 1280 + nt * 32));
          acc[nt] = __builtin_amdgcn_mfma_f32_16x16x32_bf16(wv, __builtin_bit_cast(bf16x8, xf[ks]), acc[nt], 0, 0, 0);
        }
      if (st == 1) STAMP(27); else STAMP(26);
    }
    const u32 stg = L_A3 + w * 4096;
#pragma unroll
    for (int i = 0; i < 5; ++i) *lds<u32x2>(stg + l15 * 160 + i * 32 + gq * 8) = (u32x2){pk2(acc[i][0], acc[i][1]), pk2(acc[i][2], acc[i][3])};
    lgkm0();
    asm volatile(".Lpf_end:");
    const u32 ob = (64 * g + 16 * w) * 2560 + 1280 + hq * 160;
#pragma unroll
    for (int i = 0; i < 3; ++i) {
      const u32 c = i < 2 ? i * 64 + lane : 128 + l31, r = c / 10;
      __builtin_amdgcn_raw_buffer_store_b128(*lds<u32x4>(stg + c * 16), rsOut, (i == 2 && h) ? OOB : ob + r * 2560 + (c - r * 10) * 16, 0, 0);
    }
  }
  STAMP(12);
  STAMP_OUT;
}

extern "C" void kernel_launch(void* const* d_in, const int* in_sizes, int n_in, void* d_out, int out_size, void* d_ws, size_t ws_size,
                              hipStream_t stream) {
  fused_kernel<<<256, 512, 0, stream>>>((const u16*)d_in[6], (const u16*)d_in[8], (const u16*)d_in[9], (const u16*)d_in[7], (char*)d_ws,
                                        (const u16*)d_in[1], (const u16*)d_in[2], (const u16*)d_in[3], (const u16*)d_in[4],
                                        (const u16*)d_in[0], (const u16*)d_in[5], (const u16*)d_in[10], (u16*)d_out);
}
```

```cpp
#include <hip/hip_runtime.h>
#include <hip/hip_bf16.h>

typedef unsigned short u16;
typedef unsigned int u32;
typedef unsigned long long u64;
typedef __bf16 bf16x2 __attribute__((ext_vector_type(2)));
typedef __bf16 bf16x8 __attribute__((ext_vector_type(8)));
typedef __bf16 bf16x16 __attribute__((ext_vector_type(16)));
typedef short s16x4 __attribute__((ext_vector_type(4)));
typedef float f32x2 __attribute__((ext_vector_type(2)));
typedef float f32x4 __attribute__((ext_vector_type(4)));
typedef float f32x8 __attribute__((ext_vector_type(8)));
typedef float f32x16 __attribute__((ext_vector_type(16)));
typedef u32 u32x2 __attribute__((ext_vector_type(2)));
typedef u32 u32x4 __attribute__((ext_vector_type(4)));
typedef u32 u32x4u __attribute__((ext_vector_type(4), aligned(2)));
typedef __amdgpu_buffer_rsrc_t rsrc_t;
#define LAS __attribute__((address_space(3)))
#define GAS __attribute__((address_space(1)))
#define DI __device__ __forceinline__
#define SC1 16
#define NT 2
#define OOB 0x80000000u
#define SB() __builtin_amdgcn_sched_barrier(0)

constexpr int S = 2048, D = 640;
constexpr int NREP = 8;
constexpr u32 WS_E = 0;
constexpr u32 WS_FKV = 4096;
constexpr u32 FSA = 8, FSO = 8;
constexpr u32 WS_FA1 = WS_FKV + NREP * 2048;
constexpr u32 WS_FO = WS_FA1 + 8192;
constexpr u32 WS_DBG = 65536;
constexpr u32 WS_REC = 131072;
constexpr u32 WS_KVT = 524288;
constexpr u32 HS = 327680 + 128, GS = 81920 + 128;
constexpr u32 WS_YT = WS_KVT + 8 * HS;
constexpr u32 WS_O = WS_YT + 8 * HS;
constexpr u32 WS_END = WS_O + 32 * GS;
constexpr u32 L_X = 0;
constexpr u32 L_RING = 84480;
constexpr u32 L_WK = L_RING + 40960;
constexpr u32 L_PART = L_WK + 25600;
constexpr u32 L_PAR = L_PART + 8192;
constexpr u32 L_QF = 0;
constexpr u32 L_KV = 12288;
constexpr u32 L_A1 = 34816;
constexpr u32 L_OX = 49152;
constexpr u32 L_WM = 49152;
constexpr u32 L_QT = 86016;
constexpr u32 L_CRED = 12288;
constexpr u32 L_W = 0;
constexpr u32 CV_CH = 21248;
constexpr u32 L_CNT = 163840 - 128;
constexpr u32 L_CV = L_CNT - 3 * CV_CH;
constexpr u32 L_OY = 102400;
constexpr u32 L_GO = L_OY + 36864;
constexpr u32 L_END = 163840;
static_assert(L_KV + 22080 <= L_A1 && L_GO + 22528 <= L_CNT && L_PART + 8192 <= L_CNT, "lds");
enum { C_PART, C_KT, C_CS, C_CR, C_CH, C_P0, C_P1, C_CD, C_A3, C_AT0, C_AT1, C_OR, C_PUB, C_GO, C_K0, C_K1, C_WM, C_N };
#define CNT(i) (L_CNT + 4 * (i))

#define STAMP(i) do {} while (0)
#define CSTAMP(i) do {} while (0)
#define STAMP_INIT do {} while (0)
#define STAMP_OUT do {} while (0)

#define LSTAMP(i) do {} while (0)
DI rsrc_t mkrs(const void* p, u32 bytes) { return __builtin_amdgcn_make_buffer_rsrc((void*)p, (short)0, (int)bytes, 0x00020000); }
DI u32 pk2(float a, float b) { f32x2 v = {a, b}; return __builtin_bit_cast(u32, __builtin_convertvector(v, bf16x2)); }
DI float lo2f(u32 x) { return __builtin_bit_cast(float, x << 16); }
DI float hi2f(u32 x) { return __builtin_bit_cast(float, x & 0xffff0000u); }
DI float asf(u32 x) { return __builtin_bit_cast(float, x); }
DI u32 asu(float x) { return __builtin_bit_cast(u32, x); }
DI float upper_half(float x) { asm volatile("s_nop 1" : "+v"(x)); const auto r = __builtin_amdgcn_permlane32_swap(asu(x), asu(x), false, false); return asf(r[1]); }
DI u32 umin_(u32 a, u32 b) { return a < b ? a : b; }
DI u32 umax_(u32 a, u32 b) { return a > b ? a : b; }
DI float add1(float a, float b) { float r; asm("v_add_f32 %0, %1, %2" : "=v"(r) : "v"(a), "v"(b)); return r; }
DI float mul1(float a, float b) { float r; asm("v_mul_f32 %0, %1, %2" : "=v"(r) : "v"(a), "v"(b)); return r; }
DI float fma1(float a, float b, float c) { float r; asm("v_fma_f32 %0, %1, %2, %3" : "=v"(r) : "v"(a), "v"(b), "v"(c)); return r; }
DI f32x4 add4(f32x4 a, f32x4 b) { return (f32x4){add1(a[0], b[0]), add1(a[1], b[1]), add1(a[2], b[2]), add1(a[3], b[3])}; }
DI u32 pk2s(float a, float b) { return umin_(pk2(a, b), 0xFFFFFFFEu); }
DI void lgkm0() { asm volatile("s_waitcnt lgkmcnt(0)" ::: "memory"); }
DI void vm0() { asm volatile("s_waitcnt vmcnt(0)" ::: "memory"); }
template <int N> DI void vmw() { asm volatile("s_waitcnt vmcnt(%0)" ::"n"(N) : "memory"); }
DI void bar() { asm volatile("s_waitcnt lgkmcnt(0)\n\ts_barrier" ::: "memory"); }
template <class T> DI LAS T* lds(u32 off) { return (LAS T*)(size_t)off; }
DI s16x4 trread(u32 addr) { return __builtin_bit_cast(s16x4, __builtin_amdgcn_ds_read_tr16_b64_v4i16((LAS s16x4*)(size_t)addr)); }
DI bf16x8 cat8(s16x4 a, s16x4 b) { return __builtin_bit_cast(bf16x8, __builtin_shufflevector(a, b, 0, 1, 2, 3, 4, 5, 6, 7)); }
DI void dma16(u32 lds_dst, u32 voff, rsrc_t rs, u32 soff) {
  u32 keep;
  asm volatile("s_mov_b32 %0, m0\n\ts_mov_b32 m0, %1\n\ts_nop 0\n\tbuffer_load_dwordx4 %2, %3, %4 offen lds\n\ts_mov_b32 m0, %0"
               : "=&s"(keep) : "s"(lds_dst), "v"(voff), "s"(rs), "s"(soff) : "memory");
}
DI void dma16nt(u32 lds_dst, u32 voff, rsrc_t rs, u32 soff) {
  u32 keep;
  asm volatile("s_mov_b32 %0, m0\n\ts_mov_b32 m0, %1\n\ts_nop 0\n\tbuffer_load_dwordx4 %2, %3, %4 offen nt lds\n\ts_mov_b32 m0, %0"
               : "=&s"(keep) : "s"(lds_dst), "v"(voff), "s"(rs), "s"(soff) : "memory");
}
template <int N> struct IC { static constexpr int value = N; };
template <int N, int I = 0, class F> DI void static_for(F f) { if constexpr (I < N) { f(IC<I>{}); static_for<N, I + 1>(f); } }
template <int OFF> DI void d16hi(float& x, u32 addr) { asm volatile("ds_read_u16_d16_hi %0, %1 offset:%2" : "+v"(x) : "v"(addr), "n"(OFF) : "memory"); }
DI u32 swap23(u32 x) { return (x & ~12u) | ((x & 4u) << 1) | ((x & 8u) >> 1); }
DI int mod5(int x) { return x >= 5 ? x - 5 : x; }

constexpr u32 SPIN_CAP = 1u << 17;
DI bool poll_flags(const char* p, bool active, u64 tag) {
  u64 v = tag;
  if (active) v = __hip_atomic_load((GAS u64*)p, __ATOMIC_RELAXED, __HIP_MEMORY_SCOPE_AGENT);
  return __all(v == tag);
}
template <int SLP = 1>
DI void wait_flags(const char* p, bool active, u64 tag) {
  if (__builtin_expect(!poll_flags(p, active, tag), 0)) {
#pragma unroll 1
    for (u32 spins = 0; spins < SPIN_CAP; ++spins) {
      __builtin_amdgcn_s_sleep(SLP);
      if (poll_flags(p, active, tag)) break;
    }
  }
  asm volatile("" ::: "memory");
}
DI void put_flag(char* p, u64 tag) { __hip_atomic_store((GAS u64*)p, tag, __ATOMIC_RELAXED, __HIP_MEMORY_SCOPE_AGENT); }
DI void lds_arrive(u32 cnt) {
  asm volatile("s_waitcnt lgkmcnt(0)\n\ts_mov_b64 exec, 1\n\tds_add_u32 %0, %1\n\ts_mov_b64 exec, -1" ::"v"(cnt), "v"(1u) : "memory");
}
template <int SLP = 0>
DI void lds_wait(u32 cnt, u32 target) {
  u32 v, sv, n = SPIN_CAP << 3;
  asm volatile("1:\n\tds_read_b32 %0, %3\n\ts_sub_u32 %2, %2, 1\n\ts_waitcnt lgkmcnt(0)\n\tv_readfirstlane_b32 %1, %0\n\t"
               "s_cselect_b32 %1, -1, %1\n\ts_cmp_lt_u32 %1, %4\n\ts_cbranch_scc0 2f\n\ts_sleep %5\n\ts_branch 1b\n2:"
               : "=&v"(v), "=&s"(sv), "+s"(n) : "v"(cnt), "s"(target), "n"(SLP) : "memory", "scc");
}

#define CP0 0
#define CP1 0
#define CP2 0
#define CP3 2
#define A3AUX 0
#define EPRIO 0
#define ASLEEP 8
#define PSLEEP 12
#define FAUX 0
#define SPRIO 0
#define XDMA dma16nt
#define FULLKV
#define NOTLB
#define ADELAY 0
#define EGN 1
#define HAUX 2
#define YMPOS 0
#define YMAUX 16
#define MPRIO 3
#define MFLY 2
#define MPRE 0
#define MMID 3
#define VSLEEP 3
template <int NA> struct CFrag { u32x4 a[NA], b[NA]; };
template <int NA> DI void conv_load(CFrag<NA>& f, u32 hb, u32 xb) {
#pragma unroll
  for (int a = 0; a < NA; ++a) {
    f.b[NA - 1 - a] = *(volatile LAS u32x4*)(size_t)(xb + 512 * (8 - NA + a));
    const u32x2 p = *(volatile LAS u32x2*)(size_t)(hb + 512 * a), q = *(volatile LAS u32x2*)(size_t)(hb + 512 * a + 8);
    f.a[a] = (u32x4){p[0], p[1], q[0], q[1]};
  }
}
template <int MASK, int NA> DI void conv_mma(const CFrag<NA>& f, f32x4 (&acc)[8]) {
#pragma unroll
  for (int a = 0; a < NA; ++a)
#pragma unroll
    for (int c = NA - 1; c >= 0; --c)
      if (a + c < 8 && ((MASK >> (a + c)) & 1))
        acc[a + c] = __builtin_amdgcn_mfma_f32_16x16x32_bf16(__builtin_bit_cast(bf16x8, f.a[a]), __builtin_bit_cast(bf16x8, f.b[c]), acc[a + c], 0, 0, 0);
}
template <int NM, int ND> DI void weave() {
#pragma unroll
  for (int i = 0; i < NM; ++i) {
    __builtin_amdgcn_sched_group_barrier(0x8, 1, 0);
    __builtin_amdgcn_sched_group_barrier(0x100, ND, 0);
  }
  SB();
}
constexpr int tri_0(int a) { return a * 8 - a * (a - 1) / 2; }
constexpr int tri_a(int n) { int a = 0; while (tri_0(a + 1) <= n) ++a; return a; }
DI u32 conv_xb(u32 img, int lane) { return img + 16640 + 480 - 32 * (lane & 15) + 16 * (lane >> 4); }
DI u32 conv_hb(u32 img, int lane) { const u32 e = (lane & 15) + 1 + 8 * (lane >> 4); return img + (e & 3) * 4160 + (e & ~3u) * 2; }
template <int MASK, int NA>
DI void conv_shared(u32 img, int wq4, int lane) {
  const u32 xb = conv_xb(img, lane) + wq4 * 128, hb = conv_hb(img, lane) + wq4 * 128;
  const u32 dst = L_CRED + wq4 * 5120 + (lane & 15) * 64 + (lane >> 4) * 16;
  f32x4 acc[8];
#pragma unroll
  for (int i = 0; i < 8; ++i) acc[i] = (f32x4){0.f, 0.f, 0.f, 0.f};
  CFrag<NA> f0, f1;
  conv_load(f0, hb, xb);
  SB();
  __builtin_amdgcn_s_setprio(SPRIO);
  conv_load(f1, hb + 64, xb + 64);
  conv_mma<MASK>(f0, acc);
  weave<12, 2>();
  conv_mma<MASK>(f1, acc);
  SB();
  __builtin_amdgcn_s_setprio(3);
  int n = 0;
#pragma unroll
  for (int i = 0; i < 8; ++i)
    if ((MASK >> i) & 1) *lds<f32x4>(dst + 1024 * n++) = acc[i];
}

struct Rec { u32x4 k1a, k1b, v0, v1, v2, v3; u32x2 k2a, k2b; };
#define LD128(off, so) __builtin_amdgcn_raw_buffer_load_b128(rsW, v16 + (off), so, SC1)
#define LDV(off, so) __builtin_amdgcn_raw_buffer_load_b128(rsW, vv + (off), so, SC1)
#define LD64(off, so) __builtin_amdgcn_raw_buffer_load_b64(rsW, v8 + (off), so, SC1)
#define LOAD_REC(R, so) do { \
    R.k1a = LD128(0, so); SB(); R.k2a = LD64(2048, so); SB(); R.v0 = LDV(3072, so); SB(); R.v1 = LDV(0, (so) + 4096); SB(); \
    R.k1b = LD128(1024, so); SB(); R.k2b = LD64(2560, so); SB(); R.v2 = LDV(1024, (so) + 4096); SB(); } while (0)
#define LOAD_A() asm volatile( \
    "buffer_load_dwordx4 %0, %8, %10, %11 offen sc1\n\tbuffer_load_dwordx2 %6, %9, %10, %11 offen offset:2048 sc1\n\t" \
    "buffer_load_dwordx4 %2, %13, %10, %11 offen offset:3072 sc1\n\tbuffer_load_dwordx4 %3, %13, %10, %12 offen sc1\n\t" \
    "buffer_load_dwordx4 %1, %8, %10, %11 offen offset:1024 sc1\n\tbuffer_load_dwordx2 %7, %9, %10, %11 offen offset:2560 sc1\n\t" \
    "buffer_load_dwordx4 %4, %13, %10, %12 offen offset:1024 sc1\n\tbuffer_load_dwordx4 %5, %13, %10, %12 offen offset:2048 sc1" \
    : "=&v"(A.k1a), "=&v"(A.k1b), "=&v"(A.v0), "=&v"(A.v1), "=&v"(A.v2), "=&v"(A.v3), "=&v"(A.k2a), "=&v"(A.k2b) \
    : "v"(v16), "v"(v8), "s"(rsW), "s"(rec0), "s"(rec0 + 4096), "v"(vv) : "memory")
DI f32x16 qk16(u32x4 k1, u32x4 q1) {
  f32x16 z;
#pragma unroll
  for (int i = 0; i < 16; ++i) z[i] = 0.f;
  return __builtin_amdgcn_mfma_f32_32x32x16_bf16(__builtin_bit_cast(bf16x8, k1), __builtin_bit_cast(bf16x8, q1), z, 0, 0, 0);
}
DI f32x16 qk8(u32x2 k2, u32x2 q2, f32x16 s) {
  return __builtin_amdgcn_mfma_f32_32x32x8bf16_1k(__builtin_bit_cast(s16x4, k2), __builtin_bit_cast(s16x4, q2), s, 0, 0, 0);
}
DI f32x16 pv_lo(u32x4 v, f32x16 p, f32x16 o, int idx) {
  f32x8 h = __builtin_shufflevector(p, p, 0, 1, 2, 3, 4, 5, 6, 7);
  return __builtin_amdgcn_smfmac_f32_32x32x32_bf16(__builtin_bit_cast(bf16x8, v), __builtin_bit_cast(bf16x16, h), o, idx, 0, 0);
}
DI f32x16 pv_hi(u32x4 v, f32x16 p, f32x16 o, int idx) {
  f32x8 h = __builtin_shufflevector(p, p, 8, 9, 10, 11, 12, 13, 14, 15);
  return __builtin_amdgcn_smfmac_f32_32x32x32_bf16(__builtin_bit_cast(bf16x8, v), __builtin_bit_cast(bf16x16, h), o, idx, 0, 0);
}
#define EXP4(X, i) do { X[i] = __builtin_amdgcn_exp2f(X[i]); X[i + 1] = __builtin_amdgcn_exp2f(X[i + 1]); X[i + 2] = __builtin_amdgcn_exp2f(X[i + 2]); X[i + 3] = __builtin_amdgcn_exp2f(X[i + 3]); } while (0)
#define VMWAIT(n) __builtin_amdgcn_s_waitcnt(0x0F70 | ((n) & 15) | (((n) >> 4) << 14))
#define STEPX(SI, H, HP, HN, VPREV, VCUR, K1N, K2N, QK, MID, END) do { \
    EXP4(Sx[SI], 0); SB(); \
    if ((QK) != 2) O[HP] = pv_hi(VPREV, Sx[(SI + 3) & 3], O[HP], idx); \
    SB(); MID; SB(); \
    EXP4(Sx[SI], 4); SB(); \
    if (QK) Sx[(SI + 2) & 3] = qk16(K1N, q1[HN]); \
    SB(); \
    EXP4(Sx[SI], 8); SB(); \
    if (QK) Sx[(SI + 2) & 3] = qk8(K2N, q2[HN], Sx[(SI + 2) & 3]); \
    SB(); \
    EXP4(Sx[SI], 12); SB(); \
    O[H] = pv_lo(VCUR, Sx[SI], O[H], idx); SB(); \
    END; SB(); \
  } while (0)
#define STEP(H, VPREV, VCUR, K1N, K2N, QK, WAITN, REFILL) \
    STEPX(H, H, (H + 3) & 3, (H + 2) & 3, VPREV, VCUR, K1N, K2N, QK, , if ((WAITN) >= 0) VMWAIT((WAITN) >= 0 ? (WAITN) : 0); REFILL)
#define WN(r, i) ((r) < 6 ? 10 : 10 - (i))
#define REC_STEPS(R, N, r) do { \
    const u32 sn = rec0 + ((r) + 1) * 7168 + 4096, so = sn + 3072, sp = so + 4096; \
    STEP(0, N.v3, R.v0, R.k1a, R.k2a, (r) == 0 ? 2 : 1, WN(r, 0), N.v3 = LDV(2048, sn)); \
    STEP(1, R.v1, R.v0, R.k1a, R.k2a, 1, WN(r, 1), if ((r) < 6) R.k1a = LD128(0, so)); \
    STEP(2, R.v1, R.v0, R.k1b, R.k2b, 1, -1, if ((r) < 6) R.k2a = LD64(2048, so)); \
    STEP(3, R.v1, R.v0, R.k1b, R.k2b, 1, WN(r, 3), if ((r) < 6) R.v0 = LDV(3072, so)); \
    STEP(0, R.v1, R.v2, R.k1b, R.k2b, 1, WN(r, 4), if ((r) < 6) R.v1 = LDV(0, sp)); \
    STEP(1, R.v3, R.v2, R.k1b, R.k2b, 1, WN(r, 5), if ((r) < 6) R.k1b = LD128(1024, so)); \
    STEP(2, R.v3, R.v2, N.k1a, N.k2a, 1, (r) < 6 ? -1 : 2, if ((r) < 6) R.k2b = LD64(2560, so)); \
    if ((r) < 6) STEP(3, R.v3, R.v2, N.k1a, N.k2a, 1, WN(r, 7), R.v2 = LDV(1024, sp)); \
    else STEPX(3, 3, 2, 0, R.v3, R.v2, N.k1b, N.k2b, 1, , VMWAIT(0)); \
    LSTAMP(32 + (r)); \
  } while (0)

__global__ void __launch_bounds__(512, 2)
fused_kernel(const u16* __restrict__ Bx, const u16* __restrict__ Bwk, const u16* __restrict__ Bwv, const u16* __restrict__ Bwq, char* ws,
             const u16* __restrict__ Ak, const u16* __restrict__ Av, const u16* __restrict__ Ash, const u16* __restrict__ Assm,
             const u16* __restrict__ Aq, const u16* __restrict__ AD, const u16* __restrict__ Bwo, u16* __restrict__ out) {
  __shared__ __attribute__((aligned(16))) char smem[L_END];
  asm volatile("" ::"v"((u32)(size_t)(LAS char*)smem));
  const int lane = threadIdx.x & 63;
  const int w = __builtin_amdgcn_readfirstlane(threadIdx.x >> 6);
  const int bj = blockIdx.x >> 3, g = (blockIdx.x & 7) * 4 + (bj >> 3), hq = bj & 7;
  const int gq = lane >> 4, l15 = lane & 15;
  const int half = w >> 2, wq4 = w & 3, h = lane >> 5, l31 = lane & 31;
  const int hp = (hq >> 1) & 1;
  STAMP_INIT;
  STAMP(0); CSTAMP(29);
  const rsrc_t rsW = mkrs(ws, WS_END), rsX = mkrs(Bx, S * D * 2), rsQ = mkrs(Bwq, D * D * 2);
  const rsrc_t rsK = mkrs((hq & 1) ? Bwv : Bwk, D * 20 * 2);
  asm volatile("s_nop 4" ::"s"(rsW), "s"(rsX), "s"(rsQ), "s"(rsK), "s"(Ak), "s"(Av), "s"(Ash), "s"(Assm), "s"(Aq), "s"(AD), "s"(Bwo), "s"(out));

  u32x4 q1[4];
  u32x2 q2[4];
  u64 tag;
  Rec A, B;
  f32x16 O[4];
  float kf[2][11], vf[2][8];
  auto zero_o = [&]() {
#pragma unroll
    for (int i = 0; i < 4; ++i) {
#pragma unroll
      for (int j = 0; j < 16; ++j) O[i][j] = 0.f;
      asm volatile("" : "+v"(O[i]));
    }
  };
  const u32 v16 = lane * 16, vv = (h * 32 + umin_(l31, 23)) * 16, v8 = umin_(lane, 32) * 8, rec0 = WS_REC + wq4 * 8 * 7168;
  const u32 r6 = lane / 10, vo6 = r6 * 1280 + (lane - r6 * 10) * 16 + hq * 160;
  auto tile64 = [&](u32 dst, const u16* src) {
    const rsrc_t rs = mkrs(src, S * D * 2);
    const int r0 = 64 * g + 18 * wq4;
    if (lane < 60) {
      dma16nt(dst + wq4 * 2880, vo6, rs, r0 * 1280);
      dma16nt(dst + wq4 * 2880 + 960, (wq4 == 3 && r6 > 3) ? OOB : vo6, rs, (r0 + 6) * 1280);
      if (wq4 < 3) dma16nt(dst + wq4 * 2880 + 1920, vo6, rs, (r0 + 12) * 1280);
    }
  };
  const char* kvflag = ws + WS_FKV + (blockIdx.x & (NREP - 1)) * 2048 + (wq4 * 64 + lane) * 8;
  const int rot = g & 3;
  const int rbk = hq >> 1;
  const u32 xvo = gq * 1280 + ((l15 ^ (4 * gq)) * 16);
  auto wsrc = [&](int ph) {
    const u32 c = 64 * ph + lane, p = c / 6;
    return swap23(p) * 1280 + (c - p * 6) * 16;
  };
  auto dma_w = [&](int s, int blk, int ph, u32 vo) {
    dma16(L_RING + s * 12288 + blk * 3072 + ph * 1024, vo, rsQ, (mod5(rot + s) * 128 + blk * 32) * 1280 + hq * 160 + hp * 64);
  };
  auto dma_x = [&](int s, int idx) {
    const int j = mod5(rot + s), m = (4 * rbk + 4 + idx) & 15;
    dma16(L_X + (j * 16 + m) * 1056, xvo, rsX, (64 * g + 4 * m) * 1280 + 256 * j);
  };
  auto qaddr = [&](int tile) {
    const u32 G = 4 * tile + gq, hd = G / 5, ga = G - 5 * hd, q = 16 * (wq4 & 1) + l15, slot = (hd & 1) + (wq4 & 2);
    return L_QF + ((hd >> 1) ^ hp) * 6144 + (ga < 4 ? slot * 1024 + (q + 32 * (ga & 1)) * 16 + (ga >> 1) * 8 : 4096 + slot * 512 + q * 8);
  };
  auto read_q = [&]() {
#pragma unroll
    for (int i = 0; i < 4; ++i) {
      const u32 sl = (wq4 + 1 + i) & 3;
      q1[i] = *lds<u32x4>(L_QF + half * 6144 + sl * 1024 + lane * 16);
      q2[i] = *lds<u32x2>(L_QF + half * 6144 + 4096 + sl * 512 + lane * 8);
    }
  };
  u32 xa[4];
#pragma unroll
  for (int i = 0; i < 4; ++i) xa[i] = L_X + (4 * wq4 + (l15 >> 2)) * 1056 + (l15 & 3) * 256 + gq * 16 + ((i ^ (l15 & 3)) * 64);
  {
    if (w == 0) {
      if (lane < C_N) *lds<u32>(CNT(lane)) = 0;
      lgkm0();
    }
    u64 ep;
    asm volatile("s_load_dwordx2 %0, %1, 0x0" : "=s"(ep) : "s"(ws + WS_E) : "memory");
    u32 pfs;
    if (w >= 3) {
      const int j = w - 3;
#pragma unroll
      for (int m = 0; m < 4; ++m) XDMA(L_X + (j * 16 + 4 * rbk + m) * 1056, xvo, rsX, (64 * g + 16 * rbk + 4 * m) * 1280 + 256 * j);
      const int i0 = (bj >> 1) % 5;
#pragma unroll
      for (int i = 0; i < 5; ++i) { const u32 o = j * 5120 + mod5(i0 + i) * 1024; dma16(L_WK + o, lane * 16, rsK, o); }
    }
    else {
      u64 pc;
      u32 len;
      asm volatile("s_getpc_b64 %0\n.Lpf_pc%=:\n\ts_mov_b32 %1, .Lpf_end-.Lpf_pc%=" : "=s"(pc), "=s"(len));
      pc += umin_((u32)(bj * 9 + w * 3) * 128, len - 0x104);
      asm volatile("s_load_dword %0, %1, 0x0\n\ts_load_dword %0, %1, 0x80\n\ts_load_dword %0, %1, 0x100" : "=&s"(pfs) : "s"(pc) : "memory");
      if (w == 1 && lane < 40) dma16(L_PAR, lane * 16, mkrs(Ash, D * 4 * 2), hq * 640);
      if (w == 2 && lane < 10) dma16(L_PAR + 768, lane * 16, mkrs(AD, D * 2), hq * 160);
    }
    asm volatile("s_barrier" ::: "memory");
    if (w < 3) {
      const u32 wv = wsrc(w);
#pragma unroll
      for (int i = 0; i < 4; ++i) dma_w(0, i, w, wv);
#pragma unroll
      for (int i = 0; i < 4; ++i) dma_x(0, 4 * w + i);
#pragma unroll
      for (int i = 0; i < 4; ++i) dma_x(1, 4 * w + i);
#pragma unroll
      for (int i = 0; i < 4; ++i) dma_w(1, i, w, wv);
#pragma unroll
      for (int i = 0; i < 4; ++i) dma_x(2, 4 * w + i);
    } else {
      asm volatile("s_waitcnt vmcnt(0) lgkmcnt(0)" ::: "memory");
      STAMP(14);
      const int j = w - 3;
      const u32 rb = L_WK + j * 5120 + (8 * gq + ((lane >> 2) & 3)) * 40 + 8 * (lane & 3);
      const u32 xr = L_X + (j * 16 + 4 * rbk + (l15 >> 2)) * 1056 + (l15 & 3) * 256 + gq * 16;
      f32x4 a0 = {0.f, 0.f, 0.f, 0.f}, a1v = a0;
      bf16x8 w0[4], w1[4], xk[4];
#pragma unroll
      for (int ks = 0; ks < 4; ++ks) {
        xk[ks] = *lds<bf16x8>(xr + ((ks ^ (l15 & 3)) * 64));
        w0[ks] = cat8(trread(rb + ks * 1280), trread(rb + ks * 1280 + 160));
        w1[ks] = cat8(trread(rb + ks * 1280 + 32), trread(rb + ks * 1280 + 192));
      }
      SB();
      if (hq & 1) {
#pragma unroll
        for (int ks = 0; ks < 4; ++ks) {
          a0 = __builtin_amdgcn_mfma_f32_16x16x32_bf16(xk[ks], w0[ks], a0, 0, 0, 0);
          a1v = __builtin_amdgcn_mfma_f32_16x16x32_bf16(xk[ks], w1[ks], a1v, 0, 0, 0);
        }
      } else {
#pragma unroll
        for (int ks = 0; ks < 4; ++ks) {
          a0 = __builtin_amdgcn_mfma_f32_16x16x32_bf16(w0[ks], xk[ks], a0, 0, 0, 0);
          a1v = __builtin_amdgcn_mfma_f32_16x16x32_bf16(w1[ks], xk[ks], a1v, 0, 0, 0);
        }
      }
      if (w < 7) {
        *lds<f32x4>(L_PART + j * 2048 + lane * 16) = a0;
        *lds<f32x4>(L_PART + j * 2048 + 1024 + lane * 16) = a1v;
        lds_arrive(CNT(C_PART));
      } else {
        __builtin_amdgcn_s_setprio(3);
        lds_wait(CNT(C_PART), 4);
        f32x4 p0[4], p1[4];
#pragma unroll
        for (int i = 0; i < 4; ++i) {
          p0[i] = *lds<f32x4>(L_PART + i * 2048 + lane * 16);
          p1[i] = *lds<f32x4>(L_PART + i * 2048 + 1024 + lane * 16);
        }
        SB();
        a0 += (p0[0] + p0[1]) + (p0[2] + p0[3]);
        a1v += (p1[0] + p1[1]) + (p1[2] + p1[3]);
        u32x2 o0 = {pk2(a0[0], a0[1]), pk2(a0[2], a0[3])}, o1 = {pk2(a1v[0], a1v[1]), pk2(a1v[2], a1v[3])};
        const u32 rec = WS_REC + g * 7168;
        if (hq & 1) {
          if (l15 == 4) o1 = (u32x2){0x3f803f80u, 0x3f803f80u};
          if (l15 > 4) o1 = (u32x2){0u, 0u};
          const auto x = __builtin_amdgcn_permlane16_swap(o0[0], o1[0], false, false);
          const auto y = __builtin_amdgcn_permlane16_swap(o0[1], o1[1], false, false);
          const u32 rho = (gq & 1) ? 16 + l15 : swap23(l15);
          __builtin_amdgcn_raw_buffer_store_b128((u32x4){x[0], y[0], x[1], y[1]}, rsW, rec + 3072 + rbk * 1024 + (gq >> 1) * 512 + rho * 16, 0, SC1);
        } else {
          const float c = 0.22360679774997896f * 1.4426950408889634f;
          o0 = (u32x2){pk2(lo2f(o0[0]) * c, hi2f(o0[0]) * c), pk2(lo2f(o0[1]) * c, hi2f(o0[1]) * c)};
          o1 = (u32x2){pk2(lo2f(o1[0]) * c, hi2f(o1[0]) * c), pk2(lo2f(o1[1]) * c, hi2f(o1[1]) * c)};
          if (gq == 1) o1 = (u32x2){0x3f803f80u, 0x00003f80u};
          const auto x = __builtin_amdgcn_permlane32_swap(o0[0], o0[0], false, false);
          const auto y = __builtin_amdgcn_permlane32_swap(o0[1], o0[1], false, false);
          const u32 T = rbk >> 1, key = 16 * (rbk & 1) + l15 + 32 * gq;
          __builtin_amdgcn_raw_buffer_store_b128((u32x4){x[0], y[0], x[1], y[1]}, rsW, gq < 2 ? rec + T * 1024 + key * 16 : OOB, 0, SC1);
          __builtin_amdgcn_raw_buffer_store_b64(o1, rsW, gq < 2 ? rec + 2048 + T * 512 + key * 8 : OOB, 0, SC1);
        }
        STAMP(17);
      }
    }
    asm volatile("s_waitcnt lgkmcnt(0)" : "+s"(ep) : "s"(pfs) : "memory");
    tag = ep + 0x9E3779B97F4A7C15ull;
  }
  auto put_o = [&](u32x4 o0, u32x2 o1) {
    const u32 oo = WS_O + g * GS + (((wq4 & 2) + (l31 >> 4)) * 20 + 10 * half) * 1024 + l15 * 16;
    __builtin_amdgcn_raw_buffer_store_b128(o0, rsW, oo + hq * 1024 + (2 * (wq4 & 1) + h) * 256, 0, SC1);
    __builtin_amdgcn_raw_buffer_store_b64(o1, rsW, h ? OOB : oo + (8 + (hq >> 2)) * 1024 + (hq & 3) * 256 + (wq4 & 1) * 8, 0, SC1);
  };
  auto chan = [&](int c) { return 5 * (g >> 1) + (c == 0 ? 2 : 3 * (g & 1) + c - 1); };
  if (__builtin_expect(half == 1, 0)) {
    bf16x8 xy[5][4];
    const u32 mv = (lane >> 2) * 1280 + ((lane & 3) ^ (2 * h)) * 16;
    auto mdma = [&](int i) {
      dma16(L_WM + (i >> 1) * 8192 + (2 * wq4 + (i & 1)) * 1024, mv, rsQ, (mod5(rot + (i >> 1)) * 128 + 32 * wq4 + 16 * (i & 1)) * 1280 + hq * 160 + (hp ? 0 : 96));
    };
    u32 qa0 = qaddr(hp ? 0 : 3), qa1 = qaddr(hp ? 1 : 4), wm0 = L_WM + (8 * gq + ((lane >> 2) & 3)) * 64 + 8 * (lane & 3) + (gq & 1) * 32, wm1 = wm0 ^ 32;
    asm volatile("" : "+v"(qa0), "+v"(qa1), "+v"(wm0), "+v"(wm1));
    {
      const u32 wv0 = wsrc(0), wv1 = wsrc(1), wv2 = wsrc(2);
      auto duty = [&](int b) {
        dma_w(b, wq4, 0, wv0);
        dma_w(b, wq4, 1, wv1);
        dma_w(b, wq4, 2, wv2);
        if (b < 4) {
#pragma unroll
          for (int i = 0; i < 3; ++i) dma_x(b + 1, 3 * wq4 + i);
        }
      };
      auto rdx = [&](int s) {
#pragma unroll
        for (int ks = 0; ks < 4; ++ks) xy[s][ks] = *lds<bf16x8>(xa[ks] + mod5(rot + s) * 16896);
      };
      duty(2);
      STAMP(15);
      bar();
      STAMP(16);
      if (w == 7) {
        vmw<6>();
        if (lane < NREP) put_flag(ws + WS_FKV + lane * 2048 + (g * 8 + hq) * 8, tag);
        STAMP(1);
      }
      duty(3);
      rdx(0);
      rdx(1);
      bar();
      duty(4);
      rdx(2);
#pragma unroll
      for (int b = 0; b < 2; ++b) {
#pragma unroll
        for (int j = 0; j < 11; ++j) { kf[b][j] = 0.f; asm volatile("" : "+v"(kf[b][j])); }
#pragma unroll
        for (int j = 0; j < 8; ++j) { vf[b][j] = 0.f; asm volatile("" : "+v"(vf[b][j])); }
      }
      vmw<9>();
      bar();
      rdx(3);
      vmw<3>();
      bar();
      rdx(4);
      vm0();
      bar();
      STAMP(19);
#pragma unroll
      for (int i = 0; i < MPRE; ++i) mdma(i);
    if (lane < 60) {
      const rsrc_t rsAk = mkrs(Ak, S * D * 2);
      const int rk = 64 * g - 3 + 18 * wq4;
      dma16nt(L_KV + wq4 * 2880, rk < 0 ? (r6 < 3 ? OOB : vo6 - 3840) : vo6, rsAk, rk < 0 ? 0 : rk * 1280);
      dma16nt(L_KV + wq4 * 2880 + 960, vo6, rsAk, (rk + 6) * 1280);
      dma16nt(L_KV + wq4 * 2880 + 1920, (wq4 == 3 && r6 > 0) ? OOB : vo6, rsAk, (rk + 12) * 1280);
    }
      if (h) {
        *lds<u32x2>(L_QF + 4096 + wq4 * 512 + lane * 8) = (u32x2){0u, 0u};
        *lds<u32x2>(L_QF + 6144 + 4096 + wq4 * 512 + lane * 8) = (u32x2){0u, 0u};
      }
      bar();
      STAMP(4);
    }
    __builtin_amdgcn_s_setprio(3);
    __builtin_amdgcn_s_sleep(VSLEEP);
    tile64(L_KV + 11520, Av);
    auto ymark = [&]() {
#pragma unroll
      for (int i = 0; i < 3; ++i)
        if (i < 2 || wq4 < 2)
          __builtin_amdgcn_raw_buffer_store_b128((u32x4){~0u, ~0u, ~0u, ~0u}, rsW,
              WS_YT + hq * HS + (g >> 2) * 40960 + (8 * wq4 + (lane >> 3)) * 512 + (g & 3) * 128 + (lane & 7) * 16 + i * 16384, 0, YMAUX);
    };
    u32x2 sw[3];
    u32 dd[3];
#pragma unroll
    for (int i = 0; i < 3; ++i) {
      sw[i] = *lds<u32x2>(L_PAR + (16 * (wq4 >> 1) + l15) * 8 + i * 256);
      dd[i] = *lds<u16>(L_PAR + 768 + (16 * (wq4 >> 1) + l15) * 2 + i * 64);
    }
    STAMP(40);
    vm0();
    lds_arrive(CNT(C_KT));
    const rsrc_t rsH = mkrs(Assm, D * S * 2);
    const u32 hrow = wq4 < 3 ? (hq * 80 + chan(wq4)) * 4096 : OOB;
    u32x4 ht[4], hu[4];
#pragma unroll
    for (int i = 0; i < 4; ++i) ht[i] = __builtin_amdgcn_raw_buffer_load_b128(rsH, lane * 16 + i * 1024, hrow, HAUX);
    if (YMPOS == 0) ymark();
    lds_wait<1>(CNT(C_KT), 4);
    STAMP(41);
    {
      const u32 run = 4 * (wq4 & 1) + gq;
      const u32 ka = L_KV + 8 * run * 160 + (wq4 >> 1) * 32 + 2 * l15;
      const u32 sa = L_A1 + (16 * (wq4 >> 1) + l15) * 128 + ((7 - run) ^ (l15 & 7)) * 16;
      const u32 cr = 8 * (wq4 & 1) + (lane >> 3), sl = lane & 7;
      const u32 ra = L_A1 + (16 * (wq4 >> 1) + cr) * 128 + (sl ^ (cr & 7)) * 16;
      const u32 so = WS_KVT + hq * HS + (7 - (g >> 2)) * 40960 + (16 * (wq4 >> 1) + cr) * 512 + (3 - (g & 3)) * 128 + sl * 16;
      u32x4 md[3];
      auto rd = [&](auto I) {
        constexpr int i = decltype(I)::value;
        static_for<11>([&](auto J) { d16hi<i * 64 + decltype(J)::value * 160>(kf[i & 1][decltype(J)::value], ka); });
        static_for<8>([&](auto J) { d16hi<11520 + i * 64 + decltype(J)::value * 160>(vf[i & 1][decltype(J)::value], ka); });
      };
      auto task = [&](int i) {
        float(&k)[11] = kf[i & 1];
        float(&v)[8] = vf[i & 1];
        asm volatile("s_waitcnt lgkmcnt(0)"
                     : "+v"(k[0]), "+v"(k[1]), "+v"(k[2]), "+v"(k[3]), "+v"(k[4]), "+v"(k[5]), "+v"(k[6]), "+v"(k[7]), "+v"(k[8]), "+v"(k[9]), "+v"(k[10]),
                       "+v"(v[0]), "+v"(v[1]), "+v"(v[2]), "+v"(v[3]), "+v"(v[4]), "+v"(v[5]), "+v"(v[6]), "+v"(v[7])::"memory");
      };
      auto calc = [&](int i) {
        const float(&k)[11] = kf[i & 1];
        const float f0 = lo2f(sw[i][0]), f1 = hi2f(sw[i][0]), f2 = lo2f(sw[i][1]), f3 = hi2f(sw[i][1]), dc = lo2f(dd[i]);
        float r[8];
#pragma unroll
        for (int j = 0; j < 8; ++j) r[j] = mul1(k[j], f3);
#pragma unroll
        for (int j = 0; j < 8; ++j) r[j] = fma1(k[j + 1], f2, r[j]);
#pragma unroll
        for (int j = 0; j < 8; ++j) r[j] = fma1(k[j + 2], f1, r[j]);
#pragma unroll
        for (int j = 0; j < 8; ++j) r[j] = fma1(k[j + 3], f0, r[j]);
#pragma unroll
        for (int j = 0; j < 8; ++j) r[j] = mul1(r[j], vf[i & 1][j]);
        *lds<u32x4>(sa + i * 4096) = (u32x4){pk2(r[7], r[6]), pk2(r[5], r[4]), pk2(r[3], r[2]), pk2(r[1], r[0])};
#pragma unroll
        for (int j = 0; j < 8; ++j) r[j] = mul1(r[j], dc);
        md[i] = (u32x4){pk2(r[0], r[1]), pk2(r[2], r[3]), pk2(r[4], r[5]), pk2(r[6], r[7])};
        SB();
      };
      rd(IC<0>{});
      task(0);
      rd(IC<1>{});
      calc(0);
      task(1);
      if (wq4 < 2) rd(IC<2>{});
      calc(1);
      if (wq4 < 2) {
        task(2);
        calc(2);
      }
      lgkm0();
      STAMP(42);
      if (YMPOS == 1) ymark();
      lds_arrive(CNT(C_K0) + (wq4 >> 1) * 4);
      lds_wait(CNT(C_K0) + (wq4 >> 1) * 4, 2);
#pragma unroll
      for (int i = 0; i < 3; ++i)
        if (i < 2 || wq4 < 2) __builtin_amdgcn_raw_buffer_store_b128(*lds<u32x4>(ra + i * 4096), rsW, so + i * 16384, 0, SC1);
      lds_arrive(CNT(C_K0) + (wq4 >> 1) * 4);
      lds_wait(CNT(C_K0) + (wq4 >> 1) * 4, 4);
#pragma unroll
      for (int i = 0; i < 3; ++i)
        if (i < 2 || wq4 < 2) *lds<u32x4>(sa + i * 4096) = md[i];
    }
    const u32 img = L_CV + wq4 * CV_CH;
    auto put_h = [&](u32 a, const u32x4 (&ht)[4]) {
#pragma unroll
      for (int i = 0; i < 4; ++i) {
        *lds<u32x4>(a + 32 + i * 1024) = ht[i];
        asm volatile("ds_write2_b32 %0, %1, %2 offset0:7 offset1:8\n\tds_write2_b32 %0, %3, %4 offset0:9 offset1:10"
                     ::"v"(a + 8320 + i * 1024), "v"(ht[i][0]), "v"(ht[i][1]), "v"(ht[i][2]), "v"(ht[i][3]) : "memory");
      }
    };
    asm volatile("" : "+v"(ht[3])::"memory");
#pragma unroll
    for (int i = MPRE; i < MPRE + MMID; ++i) mdma(i);
    if (wq4 < 3) {
      if (lane < 40) *lds<u32x4>(img + (lane < 8 ? (lane >> 1) * 4160 + (lane & 1) * 16 : 16640 + 4096 + (lane - 8) * 16)) = (u32x4){0u, 0u, 0u, 0u};
      lgkm0();
      if (lane == 0) {
        *lds<u16>(img + 4160 + 30) = (u16)ht[0][0];
        *lds<u16>(img + 3 * 4160 + 26) = (u16)ht[0][0];
      }
      put_h(img + lane * 16, ht);
    }
    vmw<MMID>();
    if (lane == 0) put_flag(ws + WS_FA1 + ((hq * 4 + wq4) * 32 + g) * FSA, tag);
    STAMP(2);
#pragma unroll
    for (int i = MPRE + MMID; i < 10; ++i) {
      mdma(i);
      if (i == MPRE + MMID && wq4 < 3) {
        lgkm0();
#pragma unroll
        for (int j = 0; j < 4; ++j) hu[j] = *lds<u32x4u>(img + 34 + j * 1024 + lane * 16);
      }
      if (i == 9 && wq4 < 3) put_h(img + 4160 + lane * 16, hu);
      vmw<MFLY>();
    }
    vm0();
    lds_arrive(CNT(C_WM));
    STAMP(44);
    u32x4 t[4];
    if (wq4 < 3) {
      __builtin_amdgcn_s_sleep(ADELAY);
      wait_flags<ASLEEP>(ws + WS_FA1 + ((hq * 4 + ((chan(wq4) >> 3) & 2) + h) * 32 + l31) * FSA, true, tag);
      STAMP(43);
#pragma unroll
      for (int i = 0; i < 4; ++i) t[i] = __builtin_amdgcn_raw_buffer_load_b128(rsW, h * 40960 + l31 * 16, WS_KVT + hq * HS + (2 * i) * 40960 + chan(wq4) * 512, SC1);
    }
    lds_wait<1>(CNT(C_WM), 4);
    {
      f32x4 a0 = {0.f, 0.f, 0.f, 0.f}, a1 = a0;
      bf16x8 wf[2][5][2];
      auto rdm = [&](int b) {
#pragma unroll
        for (int n = 0; n < 5; ++n) {
          wf[b & 1][n][0] = cat8(trread(wm0 + (5 * b + n) * 2048), trread(wm0 + (5 * b + n) * 2048 + 256));
          wf[b & 1][n][1] = cat8(trread(wm1 + (5 * b + n) * 2048), trread(wm1 + (5 * b + n) * 2048 + 256));
        }
        SB();
      };
      rdm(0);
      __builtin_amdgcn_s_setprio(MPRIO);
#pragma unroll
      for (int b = 0; b < 4; ++b) {
        if (b < 3) rdm(b + 1);
#pragma unroll
        for (int n = 0; n < 5; ++n) {
          a0 = __builtin_amdgcn_mfma_f32_16x16x32_bf16(wf[b & 1][n][0], xy[(5 * b + n) >> 2][(5 * b + n) & 3], a0, 0, 0, 0);
          a1 = __builtin_amdgcn_mfma_f32_16x16x32_bf16(wf[b & 1][n][1], xy[(5 * b + n) >> 2][(5 * b + n) & 3], a1, 0, 0, 0);
        }
        SB();
      }
      __builtin_amdgcn_s_setprio(3);
      *lds<u32x2>(qa0) = (u32x2){pk2(a0[0], a0[1]), pk2(a0[2], a0[3])};
      *lds<u32x2>(qa1) = (u32x2){pk2(a1[0], a1[1]), pk2(a1[2], a1[3])};
    }
    if (wq4 < 3) {
#pragma unroll
      for (int i = 0; i < 4; ++i) *lds<u32x4>(img + 16640 + lane * 16 + i * 1024) = t[i];
    }
    lds_arrive(CNT(C_CS));
    lds_wait<2>(CNT(C_CS), 4);
    STAMP(5);
    put_o((u32x4){~0u, ~0u, ~0u, ~0u}, (u32x2){~0u, ~0u});
    tile64(L_QT, Aq);
    if (g & 1) conv_shared<0x3B, 6>(L_CV, wq4, lane);
    else conv_shared<0xC4, 8>(L_CV, wq4, lane);
    lds_arrive(CNT(C_CR));
    lds_wait<1>(CNT(C_CR), 4);
    if (wq4 < 3) {
      const u32 n = 2 * wq4 + h;
      const u32 tile = (((g & 1) ? 0x54310u : 0x762u) >> (4 * n)) & 7;
      f32x4 p[8];
#pragma unroll
      for (int i = 0; i < 8; ++i) p[i] = *lds<f32x4>(L_CRED + (i >> 1) * 5120 + (i & 1) * 16 + wq4 * 2048 + lane * 32);
      SB();
      const f32x4 a = add4(add4(p[0], p[2]), add4(p[4], p[6])), b = add4(add4(p[1], p[3]), add4(p[5], p[7]));
      __builtin_amdgcn_raw_buffer_store_b128((u32x4){pk2s(a[0], a[1]), pk2s(a[2], a[3]), pk2s(b[0], b[1]), pk2s(b[2], b[3])}, rsW,
                                             n < ((g & 1) ? 5u : 3u) ? WS_YT + hq * HS + (tile) * 40960 + chan(0) * 512 + l31 * 16 : OOB, 0, SC1);
    }
    STAMP(6);
    STAMP(7); CSTAMP(30);
    LOAD_A();
    read_q();
    zero_o();
    vmw<8>();
    lds_arrive(CNT(C_CH));
    __builtin_amdgcn_s_setprio(1);
  } else {
    f32x4 acc[3];
    u32 qa[3];
#pragma unroll
    for (int i = 0; i < 3; ++i) {
      acc[i] = (f32x4){0.f, 0.f, 0.f, 0.f};
      qa[i] = qaddr(2 * hp + i);
    }
    const u32 wl = L_RING + (16 * (gq >> 1) + 4 * (gq & 1) + ((lane >> 2) & 3)) * 96 + 8 * (lane & 3);
    bf16x8 xf[3][4], wf[2][4][3];
    u64 fl = 0;
    zero_o();
#pragma unroll
    for (int s = 0; s < 5; ++s) {
      if (s == 0) vmw<8>();
      if (s == 1) vm0();
      if (s == 0) STAMP(15);
      bar();
      if (s == 0) STAMP(16);
      if (s == 1) STAMP(18);
      if (s == 4) STAMP(19);
      if (s == 3) fl = __hip_atomic_load((GAS u64*)kvflag, __ATOMIC_RELAXED, __HIP_MEMORY_SCOPE_AGENT);
      const u32 xo = mod5(rot + s + 1) * 16896;
      auto rdw = [&](int ks) {
#pragma unroll
        for (int i = 0; i < 3; ++i) {
          const u32 wb = wl + s * 12288 + ks * 3072 + i * 32;
          wf[s & 1][ks][i] = cat8(trread(wb), trread(wb + 768));
        }
        if (s == 0) xf[0][ks] = *lds<bf16x8>(xa[ks] + rot * 16896);
        if (s < 4) xf[(s + 1) % 3][ks] = *lds<bf16x8>(xa[ks] + xo);
      };
      auto mma = [&](int st, int ks) {
#pragma unroll
        for (int i = 0; i < 3; ++i) acc[i] = __builtin_amdgcn_mfma_f32_16x16x32_bf16(wf[st & 1][ks][i], xf[st % 3][ks], acc[i], 0, 0, 0);
      };
#pragma unroll
      for (int ks = 0; ks < 4; ++ks) {
        rdw(ks);
        SB();
        if (s > 0) mma(s - 1, ks);
        SB();
      }
      if (s == 3) {
        if (!__all(fl == tag)) wait_flags(kvflag, true, tag);
        asm volatile("" ::: "memory");
        STAMP(7); CSTAMP(30);
        LOAD_A();
      }
      if (s == 4) {
#pragma unroll
        for (int ks = 0; ks < 4; ++ks) mma(4, ks);
      }
    }
    STAMP(3);
#pragma unroll
    for (int i = 0; i < 3; ++i) *lds<u32x2>(qa[i]) = (u32x2){pk2(acc[i][0], acc[i][1]), pk2(acc[i][2], acc[i][3])};
    bar();
    read_q();
    __builtin_amdgcn_s_setprio(2);
    STAMP(4);
  }
  u32x4 yv[4];
  auto yload = [&]() {
#pragma unroll
    for (int i = 0; i < 4; ++i)
      yv[i] = __builtin_amdgcn_raw_buffer_load_b128(rsW, (lane >> 3) * 512 + (lane & 7) * 16,
                                                    wq4 * 4 + i < 10 ? WS_YT + hq * HS + ((g >> 2)) * 40960 + (g & 3) * 128 + (wq4 * 4 + i) * 4096 : OOB, SC1);
    SB();
  };
  {
    const int idx = (int)0xDDDDDDDDu;
    const u32 cntA = half ? CNT(C_AT1) : CNT(C_AT0);
    asm volatile("s_waitcnt vmcnt(0)" : "+v"(A.k1a), "+v"(A.k1b), "+v"(A.v0), "+v"(A.v1), "+v"(A.v2), "+v"(A.v3), "+v"(A.k2a), "+v"(A.k2b)::"memory");
    f32x16 Sx[4];
    Sx[0] = qk8(A.k2a, q2[0], qk16(A.k1a, q1[0]));
    SB();
    Sx[1] = qk8(A.k2a, q2[1], qk16(A.k1a, q1[1]));
    SB();
    LOAD_REC(B, rec0 + 7168);
    STAMP(28);
    __builtin_amdgcn_s_waitcnt(0xC07F);
    SB();
    REC_STEPS(A, B, 0); REC_STEPS(B, A, 1); REC_STEPS(A, B, 2); REC_STEPS(B, A, 3);
    REC_STEPS(A, B, 4); REC_STEPS(B, A, 5); REC_STEPS(A, B, 6);
    const u32 ored = half ? L_OY : L_OX;
    u32 ga;
    auto give = [&](int i, int j) {
      if (j == 0) { ga = ored + (((wq4 + 1 + i) & 3) * 3 + i) * 3072 + lane * 16; asm volatile("" : "+v"(ga)); }
      *lds<f32x4>(ga + j * 1024) = (f32x4){O[i][4 * j], O[i][4 * j + 1], O[i][4 * j + 2], O[i][4 * j + 3]};
    };
    u32 cdv;
    const u32 cfree = half ? CNT(C_CD) : CNT(C_CS);
    STEPX(0, 0, 3, 1, A.v3, B.v0, B.k1a, B.k2a, 1, , cdv = *(volatile LAS u32*)(size_t)cfree);
    STEPX(1, 0, 0, 1, B.v1, B.v2, B.k1b, B.k2b, 1, ,
          if (__builtin_expect(__builtin_amdgcn_readfirstlane(cdv) < 4, 0)) lds_wait<1>(cfree, 4));
    STEPX(2, 1, 0, 2, B.v3, B.v0, B.k1a, B.k2a, 1, , give(0, 0));
    STEPX(3, 1, 1, 2, B.v1, B.v2, B.k1b, B.k2b, 1, give(0, 1), give(0, 2));
    STEPX(0, 2, 1, 3, B.v3, B.v0, B.k1a, B.k2a, 1, , give(1, 0));
    STEPX(1, 2, 2, 3, B.v1, B.v2, B.k1b, B.k2b, 1, give(1, 1), give(1, 2));
    STEPX(2, 3, 2, 0, B.v3, B.v0, B.k1a, B.k2a, 0, , give(2, 0); give(2, 1); give(2, 2));
    EXP4(Sx[3], 0); SB();
    O[3] = pv_hi(B.v1, Sx[2], O[3], idx); SB();
    EXP4(Sx[3], 4); SB();
    EXP4(Sx[3], 8); SB();
    O[3] = pv_lo(B.v2, Sx[3], O[3], idx); SB();
    lds_arrive(cntA);
    EXP4(Sx[3], 12); SB();
    O[3] = pv_hi(B.v3, Sx[3], O[3], idx); SB();
    STAMP(8); CSTAMP(31);
    lds_wait(cntA, 4);
    STAMP(21);
    f32x4 pr[9], t[3];
#pragma unroll
    for (int i = 0; i < 9; ++i) pr[i] = *lds<f32x4>(ored + wq4 * 9216 + lane * 16 + (8 - i) * 1024);
    SB();
    if (half == 1) yload();
#pragma unroll
    for (int i = 0; i < 3; ++i) t[i] = add4(add4((f32x4){O[3][4 * i], O[3][4 * i + 1], O[3][4 * i + 2], O[3][4 * i + 3]}, pr[2 - i]), add4(pr[5 - i], pr[8 - i]));
    float lsum = upper_half(t[2][0]);
    if (__builtin_expect(!__all(lsum >= 5.421010862427522e-20f && lsum <= 1.8446744073709552e19f), 0)) {
      float m = -INFINITY;
#pragma unroll 1
      for (int r = 0; r < 64; ++r) {
        const u32 so = WS_REC + (r >> 1) * 7168;
        const f32x16 sa = qk8(LD64(2048, so + (r & 1) * 512), q2[3], qk16(LD128(0, so + (r & 1) * 1024), q1[3]));
#pragma unroll
        for (int i = 0; i < 16; ++i) m = fmaxf(m, sa[i]);
      }
      const auto mm = __builtin_amdgcn_permlane32_swap(__builtin_bit_cast(u32, m), __builtin_bit_cast(u32, m), false, false);
      const float M = -fmaxf(asf(mm[0]), asf(mm[1]));
      const u32 m0 = __builtin_bit_cast(u32, M) & 0xffff0000u;
      const float r1 = M - __builtin_bit_cast(float, m0);
      const u32 m1 = __builtin_bit_cast(u32, r1) & 0xffff0000u;
      const float r2 = r1 - __builtin_bit_cast(float, m1);
      const u32x2 qs = h ? (u32x2){(m0 >> 16) | m1, __builtin_bit_cast(u32, r2) >> 16} : q2[3];
      f32x16 o;
#pragma unroll
      for (int i = 0; i < 16; ++i) o[i] = 0.f;
#pragma unroll 1
      for (int r = 0; r < 64; ++r) {
        const u32 so = WS_REC + (r >> 1) * 7168;
        f32x16 p = qk8(LD64(2048, so + (r & 1) * 512), qs, qk16(LD128(0, so + (r & 1) * 1024), q1[3]));
        const u32x4 va = LDV(0, so + 3072 + (r & 1) * 2048), vb = LDV(1024, so + 3072 + (r & 1) * 2048);
        EXP4(p, 0); EXP4(p, 4); EXP4(p, 8); EXP4(p, 12);
        o = pv_hi(vb, p, pv_lo(va, p, o, idx), idx);
      }
#pragma unroll
      for (int i = 0; i < 3; ++i) t[i] = (f32x4){o[4 * i], o[4 * i + 1], o[4 * i + 2], o[4 * i + 3]};
      lsum = upper_half(o[8]);
    }
    float inv = __builtin_amdgcn_rcpf(lsum);
    asm volatile("s_nop 0" : "+v"(inv));
    auto fin = [&](float a, float b) { return pk2s(mul1(a, inv), mul1(b, inv)); };
    put_o((u32x4){fin(t[0][0], t[0][1]), fin(t[0][2], t[0][3]), fin(t[1][0], t[1][1]), fin(t[1][2], t[1][3])},
          (u32x2){fin(t[2][0], t[2][1]), fin(t[2][2], t[2][3])});
    STAMP(22);
  }
  char* const oflag = ws + WS_FO + ((g * 8 + hq) * 4 + wq4) * FSO;
  const rsrc_t rsOut = mkrs(out, S * 2 * D * 2);
  if (half == 1) {
    __builtin_amdgcn_s_sleep(PSLEEP);
    lds_arrive(CNT(C_PUB));
    STAMP(9);
    if (wq4 < 3) {
#pragma unroll 1
      for (u32 spins = 0; spins < SPIN_CAP; ++spins) {
        u32 mx = 0;
#pragma unroll
        for (int i = 0; i < 4; ++i) mx = umax_(umax_(umax_(umax_(mx, yv[i][0]), yv[i][1]), yv[i][2]), yv[i][3]);
        if (__all(mx != ~0u)) break;
        lds_wait<1>(CNT(C_A3), 8);
        yload();
      }
      lds_wait<1>(CNT(C_GO), 3);
      STAMP(23);
      const u32 scr = L_GO + wq4 * 8192 + 4096;
      u32x4 qq[4], md[4];
#pragma unroll
      for (int i = 0; i < 4; ++i) {
        qq[i] = *lds<u32x4>(scr - 4096 + i * 1024 + lane * 16);
        md[i] = *lds<u32x4>(scr + (wq4 < 2 ? i : (i & 1)) * 1024 + lane * 16);
      }
      lgkm0();
#pragma unroll
      for (int i = 0; i < 4; ++i) {
        u32x4 y2;
#pragma unroll
        for (int j = 0; j < 4; ++j) y2[j] = pk2(add1(lo2f(yv[i][j]), lo2f(md[i][j])), add1(hi2f(yv[i][j]), hi2f(md[i][j])));
        const u32 chl = 8 * i + (lane >> 3);
        if (wq4 < 2 || i < 2) *lds<u32x4>(scr + chl * 128 + ((lane & 7) ^ (chl >> 2)) * 16) = y2;
      }
      lgkm0();
      const u32 run = lane >> 3, cq = wq4 * 8 + (lane & 7);
      const u32 ob = cq < 20 ? (64 * g + 8 * run) * 2560 + hq * 160 + cq * 8 : OOB;
      u32x4 z[4];
      u32x2 res[8];
#pragma unroll
      for (int c = 0; c < 4; ++c) z[c] = *lds<u32x4>(scr + (((lane & 7) * 4 + c) & (wq4 < 2 ? 31 : 15)) * 128 + (run ^ (lane & 7)) * 16);
#pragma unroll
      for (int i = 0; i < 8; ++i) {
        float r[4];
#pragma unroll
        for (int c = 0; c < 4; ++c) {
          const u32 qv = qq[i >> 1][2 * (i & 1) + (c >> 1)];
          r[c] = mul1((i & 1) ? hi2f(z[c][i >> 1]) : lo2f(z[c][i >> 1]), (c & 1) ? hi2f(qv) : lo2f(qv));
        }
        res[i] = (u32x2){pk2(r[0], r[1]), pk2(r[2], r[3])};
      }
      lds_wait<1>(CNT(C_A3), 8);
#pragma unroll
      for (int i = 0; i < 8; ++i) __builtin_amdgcn_raw_buffer_store_b64(res[i], rsOut, ob, i * 2560, A3AUX);
    }
  } else {
    if (blockIdx.x == 0 && threadIdx.x == 0) put_flag(ws + WS_E, tag);
    lds_wait<2>(CNT(C_CH), 4);
    u32x4 kvv[4];
    u32x2 qr[8];
#pragma unroll
    for (int i = 0; i < 8; ++i) qr[i] = *lds<u32x2>(L_QT + (8 * (lane >> 3) + i) * 160 + (w * 8 + (lane & 7)) * 8);
#pragma unroll
    for (int i = 0; i < 4; ++i) {
      const u32 chl = 8 * i + (lane >> 3);
      kvv[i] = *lds<u32x4>(L_A1 + w * 4096 + chl * 128 + ((7 - (lane & 7)) ^ (chl & 7)) * 16);
    }
    SB();
    const rsrc_t rsO = mkrs(Bwo, D * D * 2);
    u32 kp2[2], jo2[2];
#pragma unroll
    for (int i = 0; i < 2; ++i) {
      const u32 c = (i ? 256 : 64 * wq4) + lane, pos = c / 10;
      kp2[i] = swap23(pos);
      jo2[i] = (c - pos * 10) * 16;
    }
    const u32 vm0_ = (kp2[0] + (kp2[0] >> 4) * 4) * 1280 + jo2[0], vm1_ = (kp2[1] + 4) * 1280 + jo2[1];
    auto trow = [&](u32 kp) { return (kp >> 3) * 80 + ((kp >> 2) & 1) * 20 + 16 + (kp & 3); };
    const u32 vt0 = (trow(kp2[0]) + (kp2[0] >> 4) * 40) * 1280 + jo2[0], vt2 = (trow(kp2[0]) + 40 - (kp2[0] >> 4) * 40) * 1280 + jo2[0];
    const u32 vt1 = trow(kp2[1]) * 1280 + jo2[1];
    auto oload = [&](u32x4 (&xf)[10], int kh) {
#pragma unroll
      for (int ks = 0; ks < 10; ++ks)
        xf[ks] = __builtin_amdgcn_raw_buffer_load_b128(rsW, lane * 16 + (ks & 3) * 1024, WS_O + g * GS + (w * 20 + 10 * kh + (ks & ~3)) * 1024, SC1);
      SB();
    };
    u32x4 xe[10];
    {
      const u32 img = L_CV + (1 + (wq4 >> 1)) * CV_CH;
      const u32 xb = conv_xb(img, lane) + (wq4 & 1) * 256, hb = conv_hb(img, lane) + (wq4 & 1) * 256;
      f32x4 acc[8];
#pragma unroll
      for (int i = 0; i < 8; ++i) acc[i] = (f32x4){0.f, 0.f, 0.f, 0.f};
      auto wo_dma = [&](int n) {
        const int q = (n / 5 + g) & 3, e = q >> 1, c = 4 * (q & 1) + (n % 5 < 4 ? n % 5 : wq4);
        if (n < 20) dma16(L_W + (10 * e + c) * 5120 + (n % 5 < 4 ? wq4 : 4) * 1024, n % 5 < 4 ? vm0_ : vm1_, rsO, (80 * c + 40 * (((c >> 1) & 1) ^ e)) * 1280 + hq * 160);
        else if (n < 24) dma16(L_W + (10 * ((n >> 1) & 1) + 8 + (n & 1)) * 5120 + wq4 * 1024, (n & 2) ? vt2 : vt0, rsO, (n & 1) * 320 * 1280 + hq * 160);
        else dma16(L_W + (10 * (wq4 >> 1) + 8 + (wq4 & 1)) * 5120 + 4096, vt1, rsO, ((wq4 & 1) * 320 + 40 * (1 - (wq4 >> 1))) * 1280 + hq * 160);
      };
      auto residue = [&](auto BB, const CFrag<8>& f, CFrag<8>& n) {
        constexpr int B = decltype(BB)::value;
        static_for<36>([&](auto NN) {
          constexpr int N = decltype(NN)::value, a = tri_a(N), c = 7 - a - (N - tri_0(a));
          acc[a + c] = __builtin_amdgcn_mfma_f32_16x16x32_bf16(__builtin_bit_cast(bf16x8, f.a[a]), __builtin_bit_cast(bf16x8, f.b[c]), acc[a + c], 0, 0, 0);
          if constexpr (B < 3 && N < 8) n.b[7 - N] = *lds<u32x4>(xb + 64 * (B + 1) + 512 * N);
          if constexpr (B < 3 && N >= 8 && N < 24) {
            const u32x2 p = *lds<u32x2>(hb + 64 * (B + 1) + 512 * ((N - 8) >> 1) + 8 * (N & 1));
            n.a[(N - 8) >> 1][2 * (N & 1)] = p[0];
            n.a[(N - 8) >> 1][2 * (N & 1) + 1] = p[1];
          }
          if constexpr (B > 0 && N % 4 == 0 && (B - 1) * 9 + N / 4 < 25) wo_dma((B - 1) * 9 + N / 4);
          SB();
        });
      };
      lds_arrive(CNT(C_OR));
      CFrag<8> f0, f1;
      conv_load(f0, hb, xb);
      SB();
      STAMP(5);
      __builtin_amdgcn_s_setprio(CP0);
      residue(IC<0>{}, f0, f1);
      __builtin_amdgcn_s_setprio(2);
      vm0();
      if (lane == 0) put_flag(oflag, tag);
      lds_wait<1>(CNT(C_OR), 4);
      const char* eflag = ws + WS_FO + ((g * 8 + (l15 >> 1)) * 4 + (w & 2) + (l15 & 1)) * FSO;
      asm volatile("" : "+v"(eflag));
      __builtin_amdgcn_s_setprio(CP1);
      residue(IC<1>{}, f1, f0);
      __builtin_amdgcn_s_setprio(CP2);
      residue(IC<2>{}, f0, f1);
      __builtin_amdgcn_s_setprio(CP3);
      residue(IC<3>{}, f1, f0);
      __builtin_amdgcn_s_setprio(2);
      const u64 ef = __hip_atomic_load((GAS u64*)eflag, __ATOMIC_RELAXED, __HIP_MEMORY_SCOPE_AGENT);
      SB();
      const u32 xch = L_CV + 4096;
      if (wq4 & 1) {
#pragma unroll
        for (int i = 0; i < 4; ++i) { *lds<f32x4>(xch + wq4 * 4096 + i * 1024 + lane * 16) = acc[i]; acc[i] = acc[4 + i]; }
      } else {
#pragma unroll
        for (int i = 0; i < 4; ++i) *lds<f32x4>(xch + wq4 * 4096 + i * 1024 + lane * 16) = acc[4 + i];
      }
      lds_arrive(CNT(C_P0) + (wq4 >> 1) * 4);
      lds_wait<1>(CNT(C_P0) + (wq4 >> 1) * 4, 2);
      const u32 stg = img + (wq4 & 1) * 2048;
#pragma unroll
      for (int i = 0; i < 4; ++i) {
        const f32x4 v = add4(acc[i], *lds<f32x4>(xch + (wq4 ^ 1) * 4096 + i * 1024 + lane * 16));
        *lds<u32x2>(stg + 512 * i + 32 * l15 + 8 * gq) = (u32x2){pk2s(v[0], v[1]), pk2s(v[2], v[3])};
      }
      lgkm0();
      const u32 yo = WS_YT + hq * HS + ((wq4 & 1) * 4 + h) * 40960 + chan(1 + (wq4 >> 1)) * 512 + l31 * 16;
      __builtin_amdgcn_raw_buffer_store_b128(*lds<u32x4>(stg + lane * 16), rsW, yo, 0, SC1);
      __builtin_amdgcn_raw_buffer_store_b128(*lds<u32x4>(stg + 1024 + lane * 16), rsW, yo + 81920, 0, SC1);
      vmw<2>();
      lds_arrive(CNT(C_CD));
      if (!__all(ef == tag)) wait_flags<2>(eflag, true, tag);
      asm volatile("" ::: "memory");
      oload(xe, 0);
    }
    STAMP(6);
    lds_wait<1>(CNT(C_CD), 4);
    if (w < 3) {
#pragma unroll
      for (int i = 0; i < 4; ++i) {
        *lds<u32x4>(L_GO + w * 8192 + i * 1024 + lane * 16) = (u32x4){qr[2 * i][0], qr[2 * i][1], qr[2 * i + 1][0], qr[2 * i + 1][1]};
        if (w < 2 || i < 2) *lds<u32x4>(L_GO + w * 8192 + 4096 + i * 1024 + lane * 16) = kvv[i];
      }
      lds_arrive(CNT(C_GO));
    }
    f32x4 acc[5];
#pragma unroll
    for (int i = 0; i < 5; ++i) acc[i] = (f32x4){0.f, 0.f, 0.f, 0.f};
    const u32 wl = L_W + (16 * (gq >> 1) + 4 * (gq & 1) + ((lane >> 2) & 3)) * 160 + 8 * (lane & 3);
    auto wfrag = [&](u32 wb, int ks, int nt) { return cat8(trread(wb + ks * 5120 + nt * 32), trread(wb + ks * 5120 + 1280 + nt * 32)); };
    {
      lds_wait<2>(CNT(C_AT1), EGN);
      STAMP(25);
      __builtin_amdgcn_s_setprio(EPRIO);
      const u32 wb = wl;
      bf16x8 wf[2][5];
#pragma unroll
      for (int nt = 0; nt < 5; ++nt) wf[0][nt] = wfrag(wb, 0, nt);
      SB();
#pragma unroll
      for (int ks = 0; ks < 10; ++ks) {
#pragma unroll
        for (int nt = 0; nt < 5; ++nt) {
          if (ks < 9) wf[(ks + 1) & 1][nt] = wfrag(wb, ks + 1, nt);
          acc[nt] = __builtin_amdgcn_mfma_f32_16x16x32_bf16(wf[ks & 1][nt], __builtin_bit_cast(bf16x8, xe[ks]), acc[nt], 0, 0, 0);
        }
        SB();
      }
      STAMP(26);
    }
    SB();
    {
      const int kh = 1;
      const u32 wb = wl + kh * 51200;
      constexpr int NPRE = 4;
      bf16x8 wf[NPRE][5];
#pragma unroll
      for (int ks = 0; ks < NPRE; ++ks)
#pragma unroll
        for (int nt = 0; nt < 5; ++nt) wf[ks][nt] = wfrag(wb, ks, nt);
      SB();
      STAMP(10);
      lds_arrive(CNT(C_A3));
      STAMP(45);
      __builtin_amdgcn_s_setprio(2);
      lds_wait<1>(CNT(C_A3), 4);
      lds_wait<1>(CNT(C_PUB), 4);
      f32x4 res[5];
#pragma unroll 1
      for (u32 spins = 0; spins < SPIN_CAP; ++spins) {
        u32x4 xf[10];
        asm volatile("" ::: "memory");
        oload(xf, kh);
        bf16x8 wl2[2][5];
#pragma unroll
        for (int ks = 0; ks < 2; ++ks)
#pragma unroll
          for (int nt = 0; nt < 5; ++nt) wl2[ks][nt] = wfrag(wb, NPRE + ks, nt);
        SB();
        u32 mx = 0;
#pragma unroll
        for (int ks = 0; ks < 10; ++ks) {
#pragma unroll
          for (int nt = 0; nt < 5; ++nt) {
            res[nt] = __builtin_amdgcn_mfma_f32_16x16x32_bf16(ks < NPRE ? wf[ks < NPRE ? ks : 0][nt] : wl2[ks & 1][nt], __builtin_bit_cast(bf16x8, xf[ks]),
                                                              ks ? res[nt] : acc[nt], 0, 0, 0);
            if (ks >= NPRE && ks < 8) wl2[ks & 1][nt] = wfrag(wb, ks + 2, nt);
          }
          mx = umax_(umax_(umax_(umax_(mx, xf[ks][0]), xf[ks][1]), xf[ks][2]), xf[ks][3]);
          SB();
        }
        if (__all(mx != ~0u)) break;
      }
      lds_arrive(CNT(C_A3));
      STAMP(11);
#pragma unroll
      for (int nt = 0; nt < 5; ++nt) acc[nt] = res[nt];
      STAMP(27);
    }
    const u32 stg = L_W + w * 4096;
#pragma unroll
    for (int i = 0; i < 5; ++i) *lds<u32x2>(stg + l15 * 160 + i * 32 + gq * 8) = (u32x2){pk2(acc[i][0], acc[i][1]), pk2(acc[i][2], acc[i][3])};
    lgkm0();
    asm volatile(".Lpf_end:");
    const u32 ob = (64 * g + 16 * w) * 2560 + 1280 + hq * 160;
#pragma unroll
    for (int i = 0; i < 3; ++i) {
      const u32 c = i < 2 ? i * 64 + lane : 128 + l31, r = c / 10;
      __builtin_amdgcn_raw_buffer_store_b128(*lds<u32x4>(stg + c * 16), rsOut, (i == 2 && h) ? OOB : ob + r * 2560 + (c - r * 10) * 16, 0, FAUX);
    }
  }
  STAMP(12);
  STAMP_OUT;
}

extern "C" void kernel_launch(void* const* d_in, const int* in_sizes, int n_in, void* d_out, int out_size, void* d_ws, size_t ws_size,
                              hipStream_t stream) {
  fused_kernel<<<256, 512, 0, stream>>>((const u16*)d_in[6], (const u16*)d_in[8], (const u16*)d_in[9], (const u16*)d_in[7], (char*)d_ws,
                                        (const u16*)d_in[1], (const u16*)d_in[2], (const u16*)d_in[3], (const u16*)d_in[4],
                                        (const u16*)d_in[0], (const u16*)d_in[5], (const u16*)d_in[10], (u16*)d_out);
}
```

```cpp
#define HP 0
#include <hip/hip_runtime.h>
#include <hip/hip_bf16.h>

typedef unsigned short u16;
typedef unsigned int u32;
typedef unsigned long long u64;
typedef __bf16 bf16x2 __attribute__((ext_vector_type(2)));
typedef __bf16 bf16x8 __attribute__((ext_vector_type(8)));
typedef __bf16 bf16x16 __attribute__((ext_vector_type(16)));
typedef short s16x4 __attribute__((ext_vector_type(4)));
typedef float f32x2 __attribute__((ext_vector_type(2)));
typedef float f32x4 __attribute__((ext_vector_type(4)));
typedef float f32x8 __attribute__((ext_vector_type(8)));
typedef float f32x16 __attribute__((ext_vector_type(16)));
typedef u32 u32x2 __attribute__((ext_vector_type(2)));
typedef u32 u32x4 __attribute__((ext_vector_type(4)));
typedef u32 u32x4u __attribute__((ext_vector_type(4), aligned(2)));
typedef __amdgpu_buffer_rsrc_t rsrc_t;
#define LAS __attribute__((address_space(3)))
#define GAS __attribute__((address_space(1)))
#define DI __device__ __forceinline__
#define SC1 16
#define NT 2
#define OOB 0x80000000u
#define SB() __builtin_amdgcn_sched_barrier(0)

constexpr int S = 2048, D = 640;
constexpr int NREP = 8;
constexpr u32 WS_E = 0;
constexpr u32 WS_FKV = 4096;
constexpr u32 FSA = 8, FSO = 8;
constexpr u32 WS_FA1 = WS_FKV + NREP * 2048;
constexpr u32 WS_FO = WS_FA1 + 8192;
constexpr u32 WS_DBG = 65536;
constexpr u32 WS_REC = 131072;
constexpr u32 WS_KVT = 524288;
constexpr u32 HS = 327680 + 128, GS = 81920 + 128;
constexpr u32 WS_YT = WS_KVT + 8 * HS;
constexpr u32 WS_O = WS_YT + 8 * HS;
constexpr u32 WS_END = WS_O + 32 * GS;
constexpr u32 L_X = 0;
constexpr u32 L_RING = 84480;
constexpr u32 L_WK = L_RING + 40960;
constexpr u32 L_PART = L_WK + 25600;
constexpr u32 L_PAR = L_PART + 8192;
constexpr u32 L_QF = 0;
constexpr u32 L_KV = 12288;
constexpr u32 L_A1 = 34816;
constexpr u32 L_OX = 49152;
constexpr u32 L_WM = 49152;
constexpr u32 L_QT = 86016;
constexpr u32 L_CRED = 12288;
constexpr u32 L_W = 0;
constexpr u32 CV_CH = 21248;
constexpr u32 L_CNT = 163840 - 128;
constexpr u32 L_CV = L_CNT - 3 * CV_CH;
constexpr u32 L_OY = 102400;
constexpr u32 L_GO = L_OY + 36864;
constexpr u32 L_END = 163840;
static_assert(L_KV + 22080 <= L_A1 && L_GO + 22528 <= L_CNT && L_PART + 8192 <= L_CNT, "lds");
enum { C_PART, C_KT, C_CS, C_CR, C_CH, C_P0, C_P1, C_CD, C_A3, C_AT0, C_AT1, C_OR, C_PUB, C_GO, C_K0, C_K1, C_WM, C_N };
#define CNT(i) (L_CNT + 4 * (i))

#define STAMP(i) do {} while (0)
#define CSTAMP(i) do {} while (0)
#define STAMP_INIT do {} while (0)
#define STAMP_OUT do {} while (0)

#define LSTAMP(i) do {} while (0)
DI rsrc_t mkrs(const void* p, u32 bytes) { return __builtin_amdgcn_make_buffer_rsrc((void*)p, (short)0, (int)bytes, 0x00020000); }
DI u32 pk2(float a, float b) { f32x2 v = {a, b}; return __builtin_bit_cast(u32, __builtin_convertvector(v, bf16x2)); }
DI float lo2f(u32 x) { return __builtin_bit_cast(float, x << 16); }
DI float hi2f(u32 x) { return __builtin_bit_cast(float, x & 0xffff0000u); }
DI float asf(u32 x) { return __builtin_bit_cast(float, x); }
DI u32 asu(float x) { return __builtin_bit_cast(u32, x); }
DI float upper_half(float x) { asm volatile("s_nop 1" : "+v"(x)); const auto r = __builtin_amdgcn_permlane32_swap(asu(x), asu(x), false, false); return asf(r[1]); }
DI u32 umin_(u32 a, u32 b) { return a < b ? a : b; }
DI u32 umax_(u32 a, u32 b) { return a > b ? a : b; }
DI float add1(float a, float b) { float r; asm("v_add_f32 %0, %1, %2" : "=v"(r) : "v"(a), "v"(b)); return r; }
DI float mul1(float a, float b) { float r; asm("v_mul_f32 %0, %1, %2" : "=v"(r) : "v"(a), "v"(b)); return r; }
DI float fma1(float a, float b, float c) { float r; asm("v_fma_f32 %0, %1, %2, %3" : "=v"(r) : "v"(a), "v"(b), "v"(c)); return r; }
DI f32x4 add4(f32x4 a, f32x4 b) { return (f32x4){add1(a[0], b[0]), add1(a[1], b[1]), add1(a[2], b[2]), add1(a[3], b[3])}; }
DI u32 pk2s(float a, float b) { return umin_(pk2(a, b), 0xFFFFFFFEu); }
DI void lgkm0() { asm volatile("s_waitcnt lgkmcnt(0)" ::: "memory"); }
DI void vm0() { asm volatile("s_waitcnt vmcnt(0)" ::: "memory"); }
template <int N> DI void vmw() { asm volatile("s_waitcnt vmcnt(%0)" ::"n"(N) : "memory"); }
DI void bar() { asm volatile("s_waitcnt lgkmcnt(0)\n\ts_barrier" ::: "memory"); }
template <class T> DI LAS T* lds(u32 off) { return (LAS T*)(size_t)off; }
DI s16x4 trread(u32 addr) { return __builtin_bit_cast(s16x4, __builtin_amdgcn_ds_read_tr16_b64_v4i16((LAS s16x4*)(size_t)addr)); }
DI bf16x8 cat8(s16x4 a, s16x4 b) { return __builtin_bit_cast(bf16x8, __builtin_shufflevector(a, b, 0, 1, 2, 3, 4, 5, 6, 7)); }
DI void dma16(u32 lds_dst, u32 voff, rsrc_t rs, u32 soff) {
  u32 keep;
  asm volatile("s_mov_b32 %0, m0\n\ts_mov_b32 m0, %1\n\ts_nop 0\n\tbuffer_load_dwordx4 %2, %3, %4 offen lds\n\ts_mov_b32 m0, %0"
               : "=&s"(keep) : "s"(lds_dst), "v"(voff), "s"(rs), "s"(soff) : "memory");
}
DI void dma16nt(u32 lds_dst, u32 voff, rsrc_t rs, u32 soff) {
  u32 keep;
  asm volatile("s_mov_b32 %0, m0\n\ts_mov_b32 m0, %1\n\ts_nop 0\n\tbuffer_load_dwordx4 %2, %3, %4 offen nt lds\n\ts_mov_b32 m0, %0"
               : "=&s"(keep) : "s"(lds_dst), "v"(voff), "s"(rs), "s"(soff) : "memory");
}
template <int N> struct IC { static constexpr int value = N; };
template <int N, int I = 0, class F> DI void static_for(F f) { if constexpr (I < N) { f(IC<I>{}); static_for<N, I + 1>(f); } }
template <int OFF> DI void d16hi(float& x, u32 addr) { asm volatile("ds_read_u16_d16_hi %0, %1 offset:%2" : "+v"(x) : "v"(addr), "n"(OFF) : "memory"); }
DI u32 swap23(u32 x) { return (x & ~12u) | ((x & 4u) << 1) | ((x & 8u) >> 1); }
DI int mod5(int x) { return x >= 5 ? x - 5 : x; }

constexpr u32 SPIN_CAP = 1u << 17;
DI bool poll_flags(const char* p, bool active, u64 tag) {
  u64 v = tag;
  if (active) v = __hip_atomic_load((GAS u64*)p, __ATOMIC_RELAXED, __HIP_MEMORY_SCOPE_AGENT);
  return __all(v == tag);
}
template <int SLP = 1>
DI void wait_flags(const char* p, bool active, u64 tag) {
  if (__builtin_expect(!poll_flags(p, active, tag), 0)) {
#pragma unroll 1
    for (u32 spins = 0; spins < SPIN_CAP; ++spins) {
      __builtin_amdgcn_s_sleep(SLP);
      if (poll_flags(p, active, tag)) break;
    }
  }
  asm volatile("" ::: "memory");
}
DI void put_flag(char* p, u64 tag) { __hip_atomic_store((GAS u64*)p, tag, __ATOMIC_RELAXED, __HIP_MEMORY_SCOPE_AGENT); }
DI void lds_arrive(u32 cnt) {
  asm volatile("s_waitcnt lgkmcnt(0)\n\ts_mov_b64 exec, 1\n\tds_add_u32 %0, %1\n\ts_mov_b64 exec, -1" ::"v"(cnt), "v"(1u) : "memory");
}
template <int SLP = 0>
DI void lds_wait(u32 cnt, u32 target) {
  u32 v, sv, n = SPIN_CAP << 3;
  asm volatile("1:\n\tds_read_b32 %0, %3\n\ts_sub_u32 %2, %2, 1\n\ts_waitcnt lgkmcnt(0)\n\tv_readfirstlane_b32 %1, %0\n\t"
               "s_cselect_b32 %1, -1, %1\n\ts_cmp_lt_u32 %1, %4\n\ts_cbranch_scc0 2f\n\ts_sleep %5\n\ts_branch 1b\n2:"
               : "=&v"(v), "=&s"(sv), "+s"(n) : "v"(cnt), "s"(target), "n"(SLP) : "memory", "scc");
}

#define CP0 0
#define CP1 0
#define CP2 0
#define CP3 2
#define A3AUX 0
#define EPRIO 0
#define ASLEEP 8
#define PSLEEP 12
#define FAUX 0
#define SPRIO 0
#define XDMA dma16nt
#define FULLKV
#define NOTLB
#define ADELAY 0
#define EGN 1
#define HAUX 2
#define YMPOS 0
#define YMAUX 16
#define MPRIO 3
#define MFLY 2
#define MPRE 0
#define MMID 3
#define VSLEEP 3
template <int NA> struct CFrag { u32x4 a[NA], b[NA]; };
template <int NA> DI void conv_load(CFrag<NA>& f, u32 hb, u32 xb) {
#pragma unroll
  for (int a = 0; a < NA; ++a) {
    f.b[NA - 1 - a] = *(volatile LAS u32x4*)(size_t)(xb + 512 * (8 - NA + a));
    const u32x2 p = *(volatile LAS u32x2*)(size_t)(hb + 512 * a), q = *(volatile LAS u32x2*)(size_t)(hb + 512 * a + 8);
    f.a[a] = (u32x4){p[0], p[1], q[0], q[1]};
  }
}
template <int MASK, int NA> DI void conv_mma(const CFrag<NA>& f, f32x4 (&acc)[8]) {
#pragma unroll
  for (int a = 0; a < NA; ++a)
#pragma unroll
    for (int c = NA - 1; c >= 0; --c)
      if (a + c < 8 && ((MASK >> (a + c)) & 1))
        acc[a + c] = __builtin_amdgcn_mfma_f32_16x16x32_bf16(__builtin_bit_cast(bf16x8, f.a[a]), __builtin_bit_cast(bf16x8, f.b[c]), acc[a + c], 0, 0, 0);
}
template <int NM, int ND> DI void weave() {
#pragma unroll
  for (int i = 0; i < NM; ++i) {
    __builtin_amdgcn_sched_group_barrier(0x8, 1, 0);
    __builtin_amdgcn_sched_group_barrier(0x100, ND, 0);
  }
  SB();
}
constexpr int tri_0(int a) { return a * 8 - a * (a - 1) / 2; }
constexpr int tri_a(int n) { int a = 0; while (tri_0(a + 1) <= n) ++a; return a; }
DI u32 conv_xb(u32 img, int lane) { return img + 16640 + 480 - 32 * (lane & 15) + 16 * (lane >> 4); }
DI u32 conv_hb(u32 img, int lane) { const u32 e = (lane & 15) + 1 + 8 * (lane >> 4); return img + (e & 3) * 4160 + (e & ~3u) * 2; }
template <int MASK, int NA>
DI void conv_shared(u32 img, int wq4, int lane) {
  const u32 xb = conv_xb(img, lane) + wq4 * 128, hb = conv_hb(img, lane) + wq4 * 128;
  const u32 dst = L_CRED + wq4 * 5120 + (lane & 15) * 64 + (lane >> 4) * 16;
  f32x4 acc[8];
#pragma unroll
  for (int i = 0; i < 8; ++i) acc[i] = (f32x4){0.f, 0.f, 0.f, 0.f};
  CFrag<NA> f0, f1;
  conv_load(f0, hb, xb);
  SB();
  __builtin_amdgcn_s_setprio(SPRIO);
  conv_load(f1, hb + 64, xb + 64);
  conv_mma<MASK>(f0, acc);
  weave<12, 2>();
  conv_mma<MASK>(f1, acc);
  SB();
  __builtin_amdgcn_s_setprio(3);
  int n = 0;
#pragma unroll
  for (int i = 0; i < 8; ++i)
    if ((MASK >> i) & 1) *lds<f32x4>(dst + 1024 * n++) = acc[i];
}

struct Rec { u32x4 k1a, k1b, v0, v1, v2, v3; u32x2 k2a, k2b; };
#define LD128(off, so) __builtin_amdgcn_raw_buffer_load_b128(rsW, v16 + (off), so, SC1)
#define LDV(off, so) __builtin_amdgcn_raw_buffer_load_b128(rsW, vv + (off), so, SC1)
#define LD64(off, so) __builtin_amdgcn_raw_buffer_load_b64(rsW, v8 + (off), so, SC1)
#define LOAD_REC(R, so) do { \
    R.k1a = LD128(0, so); SB(); R.k2a = LD64(2048, so); SB(); R.v0 = LDV(3072, so); SB(); R.v1 = LDV(0, (so) + 4096); SB(); \
    R.k1b = LD128(1024, so); SB(); R.k2b = LD64(2560, so); SB(); R.v2 = LDV(1024, (so) + 4096); SB(); } while (0)
#define LOAD_A() asm volatile( \
    "buffer_load_dwordx4 %0, %8, %10, %11 offen sc1\n\tbuffer_load_dwordx2 %6, %9, %10, %11 offen offset:2048 sc1\n\t" \
    "buffer_load_dwordx4 %2, %13, %10, %11 offen offset:3072 sc1\n\tbuffer_load_dwordx4 %3, %13, %10, %12 offen sc1\n\t" \
    "buffer_load_dwordx4 %1, %8, %10, %11 offen offset:1024 sc1\n\tbuffer_load_dwordx2 %7, %9, %10, %11 offen offset:2560 sc1\n\t" \
    "buffer_load_dwordx4 %4, %13, %10, %12 offen offset:1024 sc1\n\tbuffer_load_dwordx4 %5, %13, %10, %12 offen offset:2048 sc1" \
    : "=&v"(A.k1a), "=&v"(A.k1b), "=&v"(A.v0), "=&v"(A.v1), "=&v"(A.v2), "=&v"(A.v3), "=&v"(A.k2a), "=&v"(A.k2b) \
    : "v"(v16), "v"(v8), "s"(rsW), "s"(rec0), "s"(rec0 + 4096), "v"(vv) : "memory")
DI f32x16 qk16(u32x4 k1, u32x4 q1) {
  f32x16 z;
#pragma unroll
  for (int i = 0; i < 16; ++i) z[i] = 0.f;
  return __builtin_amdgcn_mfma_f32_32x32x16_bf16(__builtin_bit_cast(bf16x8, k1), __builtin_bit_cast(bf16x8, q1), z, 0, 0, 0);
}
DI f32x16 qk8(u32x2 k2, u32x2 q2, f32x16 s) {
  return __builtin_amdgcn_mfma_f32_32x32x8bf16_1k(__builtin_bit_cast(s16x4, k2), __builtin_bit_cast(s16x4, q2), s, 0, 0, 0);
}
DI f32x16 pv_lo(u32x4 v, f32x16 p, f32x16 o, int idx) {
  f32x8 h = __builtin_shufflevector(p, p, 0, 1, 2, 3, 4, 5, 6, 7);
  return __builtin_amdgcn_smfmac_f32_32x32x32_bf16(__builtin_bit_cast(bf16x8, v), __builtin_bit_cast(bf16x16, h), o, idx, 0, 0);
}
DI f32x16 pv_hi(u32x4 v, f32x16 p, f32x16 o, int idx) {
  f32x8 h = __builtin_shufflevector(p, p, 8, 9, 10, 11, 12, 13, 14, 15);
  return __builtin_amdgcn_smfmac_f32_32x32x32_bf16(__builtin_bit_cast(bf16x8, v), __builtin_bit_cast(bf16x16, h), o, idx, 0, 0);
}
#define EXP4(X, i) do { X[i] = __builtin_amdgcn_exp2f(X[i]); X[i + 1] = __builtin_amdgcn_exp2f(X[i + 1]); X[i + 2] = __builtin_amdgcn_exp2f(X[i + 2]); X[i + 3] = __builtin_amdgcn_exp2f(X[i + 3]); } while (0)
#define VMWAIT(n) __builtin_amdgcn_s_waitcnt(0x0F70 | ((n) & 15) | (((n) >> 4) << 14))
#define STEPX(SI, H, HP, HN, VPREV, VCUR, K1N, K2N, QK, MID, END) do { \
    EXP4(Sx[SI], 0); SB(); \
    if ((QK) != 2) O[HP] = pv_hi(VPREV, Sx[(SI + 3) & 3], O[HP], idx); \
    SB(); MID; SB(); \
    EXP4(Sx[SI], 4); SB(); \
    if (QK) Sx[(SI + 2) & 3] = qk16(K1N, q1[HN]); \
    SB(); \
    EXP4(Sx[SI], 8); SB(); \
    if (QK) Sx[(SI + 2) & 3] = qk8(K2N, q2[HN], Sx[(SI + 2) & 3]); \
    SB(); \
    EXP4(Sx[SI], 12); SB(); \
    O[H] = pv_lo(VCUR, Sx[SI], O[H], idx); SB(); \
    END; SB(); \
  } while (0)
#define STEP(H, VPREV, VCUR, K1N, K2N, QK, WAITN, REFILL) \
    STEPX(H, H, (H + 3) & 3, (H + 2) & 3, VPREV, VCUR, K1N, K2N, QK, , if ((WAITN) >= 0) VMWAIT((WAITN) >= 0 ? (WAITN) : 0); REFILL)
#define WN(r, i) ((r) < 6 ? 10 : 10 - (i))
#define REC_STEPS(R, N, r) do { \
    const u32 sn = rec0 + ((r) + 1) * 7168 + 4096, so = sn + 3072, sp = so + 4096; \
    STEP(0, N.v3, R.v0, R.k1a, R.k2a, (r) == 0 ? 2 : 1, WN(r, 0), N.v3 = LDV(2048, sn)); \
    STEP(1, R.v1, R.v0, R.k1a, R.k2a, 1, WN(r, 1), if ((r) < 6) R.k1a = LD128(0, so)); \
    STEP(2, R.v1, R.v0, R.k1b, R.k2b, 1, -1, if ((r) < 6) R.k2a = LD64(2048, so)); \
    STEP(3, R.v1, R.v0, R.k1b, R.k2b, 1, WN(r, 3), if ((r) < 6) R.v0 = LDV(3072, so)); \
    STEP(0, R.v1, R.v2, R.k1b, R.k2b, 1, WN(r, 4), if ((r) < 6) R.v1 = LDV(0, sp)); \
    STEP(1, R.v3, R.v2, R.k1b, R.k2b, 1, WN(r, 5), if ((r) < 6) R.k1b = LD128(1024, so)); \
    STEP(2, R.v3, R.v2, N.k1a, N.k2a, 1, (r) < 6 ? -1 : 2, if ((r) < 6) R.k2b = LD64(2560, so)); \
    if ((r) < 6) STEP(3, R.v3, R.v2, N.k1a, N.k2a, 1, WN(r, 7), R.v2 = LDV(1024, sp)); \
    else STEPX(3, 3, 2, 0, R.v3, R.v2, N.k1b, N.k2b, 1, , VMWAIT(0)); \
    LSTAMP(32 + (r)); \
  } while (0)

__global__ void __launch_bounds__(512, 2)
fused_kernel(const u16* __restrict__ Bx, const u16* __restrict__ Bwk, const u16* __restrict__ Bwv, const u16* __restrict__ Bwq, char* ws,
             const u16* __restrict__ Ak, const u16* __restrict__ Av, const u16* __restrict__ Ash, const u16* __restrict__ Assm,
             const u16* __restrict__ Aq, const u16* __restrict__ AD, const u16* __restrict__ Bwo, u16* __restrict__ out) {
  __shared__ __attribute__((aligned(16))) char smem[L_END];
  asm volatile("" ::"v"((u32)(size_t)(LAS char*)smem));
  const int lane = threadIdx.x & 63;
  const int w = __builtin_amdgcn_readfirstlane(threadIdx.x >> 6);
  const int bj = blockIdx.x >> 3, g = (blockIdx.x & 7) * 4 + (bj >> 3), hq = bj & 7;
  const int gq = lane >> 4, l15 = lane & 15;
  const int half = w >> 2, wq4 = w & 3, h = lane >> 5, l31 = lane & 31;
  const int hp = (hq >> 1) & 1;
  STAMP_INIT;
  u64 tstart; asm volatile("s_memrealtime %0\n\ts_waitcnt lgkmcnt(0)" : "=s"(tstart));
#define HOLD(k, T) do { if (HP == k) { lgkm0(); while (__builtin_amdgcn_s_memrealtime() - tstart < T) __builtin_amdgcn_s_sleep(1); asm volatile("" ::: "memory"); } } while (0)
  STAMP(0); CSTAMP(29);
  const rsrc_t rsW = mkrs(ws, WS_END), rsX = mkrs(Bx, S * D * 2), rsQ = mkrs(Bwq, D * D * 2);
  const rsrc_t rsK = mkrs((hq & 1) ? Bwv : Bwk, D * 20 * 2);
  asm volatile("s_nop 4" ::"s"(rsW), "s"(rsX), "s"(rsQ), "s"(rsK), "s"(Ak), "s"(Av), "s"(Ash), "s"(Assm), "s"(Aq), "s"(AD), "s"(Bwo), "s"(out));

  u32x4 q1[4];
  u32x2 q2[4];
  u64 tag;
  Rec A, B;
  f32x16 O[4];
  float kf[2][11], vf[2][8];
  auto zero_o = [&]() {
#pragma unroll
    for (int i = 0; i < 4; ++i) {
#pragma unroll
      for (int j = 0; j < 16; ++j) O[i][j] = 0.f;
      asm volatile("" : "+v"(O[i]));
    }
  };
  const u32 v16 = lane * 16, vv = (h * 32 + umin_(l31, 23)) * 16, v8 = umin_(lane, 32) * 8, rec0 = WS_REC + wq4 * 8 * 7168;
  const u32 r6 = lane / 10, vo6 = r6 * 1280 + (lane - r6 * 10) * 16 + hq * 160;
  auto tile64 = [&](u32 dst, const u16* src) {
    const rsrc_t rs = mkrs(src, S * D * 2);
    const int r0 = 64 * g + 18 * wq4;
    if (lane < 60) {
      dma16nt(dst + wq4 * 2880, vo6, rs, r0 * 1280);
      dma16nt(dst + wq4 * 2880 + 960, (wq4 == 3 && r6 > 3) ? OOB : vo6, rs, (r0 + 6) * 1280);
      if (wq4 < 3) dma16nt(dst + wq4 * 2880 + 1920, vo6, rs, (r0 + 12) * 1280);
    }
  };
  const char* kvflag = ws + WS_FKV + (blockIdx.x & (NREP - 1)) * 2048 + (wq4 * 64 + lane) * 8;
  const int rot = g & 3;
  const int rbk = hq >> 1;
  const u32 xvo = gq * 1280 + ((l15 ^ (4 * gq)) * 16);
  auto wsrc = [&](int ph) {
    const u32 c = 64 * ph + lane, p = c / 6;
    return swap23(p) * 1280 + (c - p * 6) * 16;
  };
  auto dma_w = [&](int s, int blk, int ph, u32 vo) {
    dma16(L_RING + s * 12288 + blk * 3072 + ph * 1024, vo, rsQ, (mod5(rot + s) * 128 + blk * 32) * 1280 + hq * 160 + hp * 64);
  };
  auto dma_x = [&](int s, int idx) {
    const int j = mod5(rot + s), m = (4 * rbk + 4 + idx) & 15;
    dma16(L_X + (j * 16 + m) * 1056, xvo, rsX, (64 * g + 4 * m) * 1280 + 256 * j);
  };
  auto qaddr = [&](int tile) {
    const u32 G = 4 * tile + gq, hd = G / 5, ga = G - 5 * hd, q = 16 * (wq4 & 1) + l15, slot = (hd & 1) + (wq4 & 2);
    return L_QF + ((hd >> 1) ^ hp) * 6144 + (ga < 4 ? slot * 1024 + (q + 32 * (ga & 1)) * 16 + (ga >> 1) * 8 : 4096 + slot * 512 + q * 8);
  };
  auto read_q = [&]() {
#pragma unroll
    for (int i = 0; i < 4; ++i) {
      const u32 sl = (wq4 + 1 + i) & 3;
      q1[i] = *lds<u32x4>(L_QF + half * 6144 + sl * 1024 + lane * 16);
      q2[i] = *lds<u32x2>(L_QF + half * 6144 + 4096 + sl * 512 + lane * 8);
    }
  };
  u32 xa[4];
#pragma unroll
  for (int i = 0; i < 4; ++i) xa[i] = L_X + (4 * wq4 + (l15 >> 2)) * 1056 + (l15 & 3) * 256 + gq * 16 + ((i ^ (l15 & 3)) * 64);
  {
    if (w == 0) {
      if (lane < C_N) *lds<u32>(CNT(lane)) = 0;
      lgkm0();
    }
    u64 ep;
    asm volatile("s_load_dwordx2 %0, %1, 0x0" : "=s"(ep) : "s"(ws + WS_E) : "memory");
    u32 pfs;
    if (w >= 3) {
      const int j = w - 3;
#pragma unroll
      for (int m = 0; m < 4; ++m) XDMA(L_X + (j * 16 + 4 * rbk + m) * 1056, xvo, rsX, (64 * g + 16 * rbk + 4 * m) * 1280 + 256 * j);
      const int i0 = (bj >> 1) % 5;
#pragma unroll
      for (int i = 0; i < 5; ++i) { const u32 o = j * 5120 + mod5(i0 + i) * 1024; dma16(L_WK + o, lane * 16, rsK, o); }
    }
    else {
      u64 pc;
      u32 len;
      asm volatile("s_getpc_b64 %0\n.Lpf_pc%=:\n\ts_mov_b32 %1, .Lpf_end-.Lpf_pc%=" : "=s"(pc), "=s"(len));
      pc += umin_((u32)(bj * 9 + w * 3) * 128, len - 0x104);
      asm volatile("s_load_dword %0, %1, 0x0\n\ts_load_dword %0, %1, 0x80\n\ts_load_dword %0, %1, 0x100" : "=&s"(pfs) : "s"(pc) : "memory");
      if (w == 1 && lane < 40) dma16(L_PAR, lane * 16, mkrs(Ash, D * 4 * 2), hq * 640);
      if (w == 2 && lane < 10) dma16(L_PAR + 768, lane * 16, mkrs(AD, D * 2), hq * 160);
    }
    asm volatile("s_barrier" ::: "memory");
    if (w < 3) {
      const u32 wv = wsrc(w);
#pragma unroll
      for (int i = 0; i < 4; ++i) dma_w(0, i, w, wv);
#pragma unroll
      for (int i = 0; i < 4; ++i) dma_x(0, 4 * w + i);
#pragma unroll
      for (int i = 0; i < 4; ++i) dma_x(1, 4 * w + i);
#pragma unroll
      for (int i = 0; i < 4; ++i) dma_w(1, i, w, wv);
#pragma unroll
      for (int i = 0; i < 4; ++i) dma_x(2, 4 * w + i);
    } else {
      asm volatile("s_waitcnt vmcnt(0) lgkmcnt(0)" ::: "memory");
      STAMP(14);
      const int j = w - 3;
      const u32 rb = L_WK + j * 5120 + (8 * gq + ((lane >> 2) & 3)) * 40 + 8 * (lane & 3);
      const u32 xr = L_X + (j * 16 + 4 * rbk + (l15 >> 2)) * 1056 + (l15 & 3) * 256 + gq * 16;
      f32x4 a0 = {0.f, 0.f, 0.f, 0.f}, a1v = a0;
      bf16x8 w0[4], w1[4], xk[4];
#pragma unroll
      for (int ks = 0; ks < 4; ++ks) {
        xk[ks] = *lds<bf16x8>(xr + ((ks ^ (l15 & 3)) * 64));
        w0[ks] = cat8(trread(rb + ks * 1280), trread(rb + ks * 1280 + 160));
        w1[ks] = cat8(trread(rb + ks * 1280 + 32), trread(rb + ks * 1280 + 192));
      }
      SB();
      if (hq & 1) {
#pragma unroll
        for (int ks = 0; ks < 4; ++ks) {
          a0 = __builtin_amdgcn_mfma_f32_16x16x32_bf16(xk[ks], w0[ks], a0, 0, 0, 0);
          a1v = __builtin_amdgcn_mfma_f32_16x16x32_bf16(xk[ks], w1[ks], a1v, 0, 0, 0);
        }
      } else {
#pragma unroll
        for (int ks = 0; ks < 4; ++ks) {
          a0 = __builtin_amdgcn_mfma_f32_16x16x32_bf16(w0[ks], xk[ks], a0, 0, 0, 0);
          a1v = __builtin_amdgcn_mfma_f32_16x16x32_bf16(w1[ks], xk[ks], a1v, 0, 0, 0);
        }
      }
      if (w < 7) {
        *lds<f32x4>(L_PART + j * 2048 + lane * 16) = a0;
        *lds<f32x4>(L_PART + j * 2048 + 1024 + lane * 16) = a1v;
        lds_arrive(CNT(C_PART));
      } else {
        __builtin_amdgcn_s_setprio(3);
        lds_wait(CNT(C_PART), 4);
        f32x4 p0[4], p1[4];
#pragma unroll
        for (int i = 0; i < 4; ++i) {
          p0[i] = *lds<f32x4>(L_PART + i * 2048 + lane * 16);
          p1[i] = *lds<f32x4>(L_PART + i * 2048 + 1024 + lane * 16);
        }
        SB();
        a0 += (p0[0] + p0[1]) + (p0[2] + p0[3]);
        a1v += (p1[0] + p1[1]) + (p1[2] + p1[3]);
        u32x2 o0 = {pk2(a0[0], a0[1]), pk2(a0[2], a0[3])}, o1 = {pk2(a1v[0], a1v[1]), pk2(a1v[2], a1v[3])};
        const u32 rec = WS_REC + g * 7168;
        if (hq & 1) {
          if (l15 == 4) o1 = (u32x2){0x3f803f80u, 0x3f803f80u};
          if (l15 > 4) o1 = (u32x2){0u, 0u};
          const auto x = __builtin_amdgcn_permlane16_swap(o0[0], o1[0], false, false);
          const auto y = __builtin_amdgcn_permlane16_swap(o0[1], o1[1], false, false);
          const u32 rho = (gq & 1) ? 16 + l15 : swap23(l15);
          __builtin_amdgcn_raw_buffer_store_b128((u32x4){x[0], y[0], x[1], y[1]}, rsW, rec + 3072 + rbk * 1024 + (gq >> 1) * 512 + rho * 16, 0, SC1);
        } else {
          const float c = 0.22360679774997896f * 1.4426950408889634f;
          o0 = (u32x2){pk2(lo2f(o0[0]) * c, hi2f(o0[0]) * c), pk2(lo2f(o0[1]) * c, hi2f(o0[1]) * c)};
          o1 = (u32x2){pk2(lo2f(o1[0]) * c, hi2f(o1[0]) * c), pk2(lo2f(o1[1]) * c, hi2f(o1[1]) * c)};
          if (gq == 1) o1 = (u32x2){0x3f803f80u, 0x00003f80u};
          const auto x = __builtin_amdgcn_permlane32_swap(o0[0], o0[0], false, false);
          const auto y = __builtin_amdgcn_permlane32_swap(o0[1], o0[1], false, false);
          const u32 T = rbk >> 1, key = 16 * (rbk & 1) + l15 + 32 * gq;
          __builtin_amdgcn_raw_buffer_store_b128((u32x4){x[0], y[0], x[1], y[1]}, rsW, gq < 2 ? rec + T * 1024 + key * 16 : OOB, 0, SC1);
          __builtin_amdgcn_raw_buffer_store_b64(o1, rsW, gq < 2 ? rec + 2048 + T * 512 + key * 8 : OOB, 0, SC1);
        }
        STAMP(17);
      }
    }
    asm volatile("s_waitcnt lgkmcnt(0)" : "+s"(ep) : "s"(pfs) : "memory");
    tag = ep + 0x9E3779B97F4A7C15ull;
  }
  auto put_o = [&](u32x4 o0, u32x2 o1) {
    const u32 oo = WS_O + g * GS + (((wq4 & 2) + (l31 >> 4)) * 20 + 10 * half) * 1024 + l15 * 16;
    __builtin_amdgcn_raw_buffer_store_b128(o0, rsW, oo + hq * 1024 + (2 * (wq4 & 1) + h) * 256, 0, SC1);
    __builtin_amdgcn_raw_buffer_store_b64(o1, rsW, h ? OOB : oo + (8 + (hq >> 2)) * 1024 + (hq & 3) * 256 + (wq4 & 1) * 8, 0, SC1);
  };
  auto chan = [&](int c) { return 5 * (g >> 1) + (c == 0 ? 2 : 3 * (g & 1) + c - 1); };
  if (__builtin_expect(half == 1, 0)) {
    bf16x8 xy[5][4];
    const u32 mv = (lane >> 2) * 1280 + ((lane & 3) ^ (2 * h)) * 16;
    auto mdma = [&](int i) {
      dma16(L_WM + (i >> 1) * 8192 + (2 * wq4 + (i & 1)) * 1024, mv, rsQ, (mod5(rot + (i >> 1)) * 128 + 32 * wq4 + 16 * (i & 1)) * 1280 + hq * 160 + (hp ? 0 : 96));
    };
    u32 qa0 = qaddr(hp ? 0 : 3), qa1 = qaddr(hp ? 1 : 4), wm0 = L_WM + (8 * gq + ((lane >> 2) & 3)) * 64 + 8 * (lane & 3) + (gq & 1) * 32, wm1 = wm0 ^ 32;
    asm volatile("" : "+v"(qa0), "+v"(qa1), "+v"(wm0), "+v"(wm1));
    {
      const u32 wv0 = wsrc(0), wv1 = wsrc(1), wv2 = wsrc(2);
      auto duty = [&](int b) {
        dma_w(b, wq4, 0, wv0);
        dma_w(b, wq4, 1, wv1);
        dma_w(b, wq4, 2, wv2);
        if (b < 4) {
#pragma unroll
          for (int i = 0; i < 3; ++i) dma_x(b + 1, 3 * wq4 + i);
        }
      };
      auto rdx = [&](int s) {
#pragma unroll
        for (int ks = 0; ks < 4; ++ks) xy[s][ks] = *lds<bf16x8>(xa[ks] + mod5(rot + s) * 16896);
      };
      duty(2);
      HOLD(2, 1500);
      bar();
      STAMP(16);
      if (w == 7) {
        vmw<6>();
        if (lane < NREP) put_flag(ws + WS_FKV + lane * 2048 + (g * 8 + hq) * 8, tag);
        STAMP(1);
      }
      duty(3);
      rdx(0);
      rdx(1);
      bar();
      duty(4);
      rdx(2);
#pragma unroll
      for (int b = 0; b < 2; ++b) {
#pragma unroll
        for (int j = 0; j < 11; ++j) { kf[b][j] = 0.f; asm volatile("" : "+v"(kf[b][j])); }
#pragma unroll
        for (int j = 0; j < 8; ++j) { vf[b][j] = 0.f; asm volatile("" : "+v"(vf[b][j])); }
      }
      vmw<9>();
      bar();
      rdx(3);
      vmw<3>();
      bar();
      rdx(4);
      vm0();
      HOLD(3, 1500);
      bar();
      STAMP(19);
#pragma unroll
      for (int i = 0; i < MPRE; ++i) mdma(i);
    if (lane < 60) {
      const rsrc_t rsAk = mkrs(Ak, S * D * 2);
      const int rk = 64 * g - 3 + 18 * wq4;
      dma16nt(L_KV + wq4 * 2880, rk < 0 ? (r6 < 3 ? OOB : vo6 - 3840) : vo6, rsAk, rk < 0 ? 0 : rk * 1280);
      dma16nt(L_KV + wq4 * 2880 + 960, vo6, rsAk, (rk + 6) * 1280);
      dma16nt(L_KV + wq4 * 2880 + 1920, (wq4 == 3 && r6 > 0) ? OOB : vo6, rsAk, (rk + 12) * 1280);
    }
      if (h) {
        *lds<u32x2>(L_QF + 4096 + wq4 * 512 + lane * 8) = (u32x2){0u, 0u};
        *lds<u32x2>(L_QF + 6144 + 4096 + wq4 * 512 + lane * 8) = (u32x2){0u, 0u};
      }
      bar();
      STAMP(4);
    }
    __builtin_amdgcn_s_setprio(3);
    __builtin_amdgcn_s_sleep(VSLEEP);
    tile64(L_KV + 11520, Av);
    auto ymark = [&]() {
#pragma unroll
      for (int i = 0; i < 3; ++i)
        if (i < 2 || wq4 < 2)
          __builtin_amdgcn_raw_buffer_store_b128((u32x4){~0u, ~0u, ~0u, ~0u}, rsW,
              WS_YT + hq * HS + (g >> 2) * 40960 + (8 * wq4 + (lane >> 3)) * 512 + (g & 3) * 128 + (lane & 7) * 16 + i * 16384, 0, YMAUX);
    };
    u32x2 sw[3];
    u32 dd[3];
#pragma unroll
    for (int i = 0; i < 3; ++i) {
      sw[i] = *lds<u32x2>(L_PAR + (16 * (wq4 >> 1) + l15) * 8 + i * 256);
      dd[i] = *lds<u16>(L_PAR + 768 + (16 * (wq4 >> 1) + l15) * 2 + i * 64);
    }
    STAMP(40);
    vm0();
    lds_arrive(CNT(C_KT));
    const rsrc_t rsH = mkrs(Assm, D * S * 2);
    const u32 hrow = wq4 < 3 ? (hq * 80 + chan(wq4)) * 4096 : OOB;
    u32x4 ht[4], hu[4];
#pragma unroll
    for (int i = 0; i < 4; ++i) ht[i] = __builtin_amdgcn_raw_buffer_load_b128(rsH, lane * 16 + i * 1024, hrow, HAUX);
    if (YMPOS == 0) ymark();
    lds_wait<1>(CNT(C_KT), 4);
    STAMP(41);
    {
      const u32 run = 4 * (wq4 & 1) + gq;
      const u32 ka = L_KV + 8 * run * 160 + (wq4 >> 1) * 32 + 2 * l15;
      const u32 sa = L_A1 + (16 * (wq4 >> 1) + l15) * 128 + ((7 - run) ^ (l15 & 7)) * 16;
      const u32 cr = 8 * (wq4 & 1) + (lane >> 3), sl = lane & 7;
      const u32 ra = L_A1 + (16 * (wq4 >> 1) + cr) * 128 + (sl ^ (cr & 7)) * 16;
      const u32 so = WS_KVT + hq * HS + (7 - (g >> 2)) * 40960 + (16 * (wq4 >> 1) + cr) * 512 + (3 - (g & 3)) * 128 + sl * 16;
      u32x4 md[3];
      auto rd = [&](auto I) {
        constexpr int i = decltype(I)::value;
        static_for<11>([&](auto J) { d16hi<i * 64 + decltype(J)::value * 160>(kf[i & 1][decltype(J)::value], ka); });
        static_for<8>([&](auto J) { d16hi<11520 + i * 64 + decltype(J)::value * 160>(vf[i & 1][decltype(J)::value], ka); });
      };
      auto task = [&](int i) {
        float(&k)[11] = kf[i & 1];
        float(&v)[8] = vf[i & 1];
        asm volatile("s_waitcnt lgkmcnt(0)"
                     : "+v"(k[0]), "+v"(k[1]), "+v"(k[2]), "+v"(k[3]), "+v"(k[4]), "+v"(k[5]), "+v"(k[6]), "+v"(k[7]), "+v"(k[8]), "+v"(k[9]), "+v"(k[10]),
                       "+v"(v[0]), "+v"(v[1]), "+v"(v[2]), "+v"(v[3]), "+v"(v[4]), "+v"(v[5]), "+v"(v[6]), "+v"(v[7])::"memory");
      };
      auto calc = [&](int i) {
        const float(&k)[11] = kf[i & 1];
        const float f0 = lo2f(sw[i][0]), f1 = hi2f(sw[i][0]), f2 = lo2f(sw[i][1]), f3 = hi2f(sw[i][1]), dc = lo2f(dd[i]);
        float r[8];
#pragma unroll
        for (int j = 0; j < 8; ++j) r[j] = mul1(k[j], f3);
#pragma unroll
        for (int j = 0; j < 8; ++j) r[j] = fma1(k[j + 1], f2, r[j]);
#pragma unroll
        for (int j = 0; j < 8; ++j) r[j] = fma1(k[j + 2], f1, r[j]);
#pragma unroll
        for (int j = 0; j < 8; ++j) r[j] = fma1(k[j + 3], f0, r[j]);
#pragma unroll
        for (int j = 0; j < 8; ++j) r[j] = mul1(r[j], vf[i & 1][j]);
        *lds<u32x4>(sa + i * 4096) = (u32x4){pk2(r[7], r[6]), pk2(r[5], r[4]), pk2(r[3], r[2]), pk2(r[1], r[0])};
#pragma unroll
        for (int j = 0; j < 8; ++j) r[j] = mul1(r[j], dc);
        md[i] = (u32x4){pk2(r[0], r[1]), pk2(r[2], r[3]), pk2(r[4], r[5]), pk2(r[6], r[7])};
        SB();
      };
      rd(IC<0>{});
      task(0);
      rd(IC<1>{});
      calc(0);
      task(1);
      if (wq4 < 2) rd(IC<2>{});
      calc(1);
      if (wq4 < 2) {
        task(2);
        calc(2);
      }
      lgkm0();
      STAMP(42);
      if (YMPOS == 1) ymark();
      lds_arrive(CNT(C_K0) + (wq4 >> 1) * 4);
      lds_wait(CNT(C_K0) + (wq4 >> 1) * 4, 2);
#pragma unroll
      for (int i = 0; i < 3; ++i)
        if (i < 2 || wq4 < 2) __builtin_amdgcn_raw_buffer_store_b128(*lds<u32x4>(ra + i * 4096), rsW, so + i * 16384, 0, SC1);
      lds_arrive(CNT(C_K0) + (wq4 >> 1) * 4);
      lds_wait(CNT(C_K0) + (wq4 >> 1) * 4, 4);
#pragma unroll
      for (int i = 0; i < 3; ++i)
        if (i < 2 || wq4 < 2) *lds<u32x4>(sa + i * 4096) = md[i];
    }
    const u32 img = L_CV + wq4 * CV_CH;
    auto put_h = [&](u32 a, const u32x4 (&ht)[4]) {
#pragma unroll
      for (int i = 0; i < 4; ++i) {
        *lds<u32x4>(a + 32 + i * 1024) = ht[i];
        asm volatile("ds_write2_b32 %0, %1, %2 offset0:7 offset1:8\n\tds_write2_b32 %0, %3, %4 offset0:9 offset1:10"
                     ::"v"(a + 8320 + i * 1024), "v"(ht[i][0]), "v"(ht[i][1]), "v"(ht[i][2]), "v"(ht[i][3]) : "memory");
      }
    };
    asm volatile("" : "+v"(ht[3])::"memory");
#pragma unroll
    for (int i = MPRE; i < MPRE + MMID; ++i) mdma(i);
    if (wq4 < 3) {
      if (lane < 40) *lds<u32x4>(img + (lane < 8 ? (lane >> 1) * 4160 + (lane & 1) * 16 : 16640 + 4096 + (lane - 8) * 16)) = (u32x4){0u, 0u, 0u, 0u};
      lgkm0();
      if (lane == 0) {
        *lds<u16>(img + 4160 + 30) = (u16)ht[0][0];
        *lds<u16>(img + 3 * 4160 + 26) = (u16)ht[0][0];
      }
      put_h(img + lane * 16, ht);
    }
    vmw<MMID>();
    if (lane == 0) put_flag(ws + WS_FA1 + ((hq * 4 + wq4) * 32 + g) * FSA, tag);
    STAMP(2);
#pragma unroll
    for (int i = MPRE + MMID; i < 10; ++i) {
      mdma(i);
      if (i == MPRE + MMID && wq4 < 3) {
        lgkm0();
#pragma unroll
        for (int j = 0; j < 4; ++j) hu[j] = *lds<u32x4u>(img + 34 + j * 1024 + lane * 16);
      }
      if (i == 9 && wq4 < 3) put_h(img + 4160 + lane * 16, hu);
      vmw<MFLY>();
    }
    vm0();
    lds_arrive(CNT(C_WM));
    STAMP(44);
    u32x4 t[4];
    if (wq4 < 3) {
      __builtin_amdgcn_s_sleep(ADELAY);
      wait_flags<ASLEEP>(ws + WS_FA1 + ((hq * 4 + ((chan(wq4) >> 3) & 2) + h) * 32 + l31) * FSA, true, tag);
      STAMP(43);
#pragma unroll
      for (int i = 0; i < 4; ++i) t[i] = __builtin_amdgcn_raw_buffer_load_b128(rsW, h * 40960 + l31 * 16, WS_KVT + hq * HS + (2 * i) * 40960 + chan(wq4) * 512, SC1);
    }
    lds_wait<1>(CNT(C_WM), 4);
    {
      f32x4 a0 = {0.f, 0.f, 0.f, 0.f}, a1 = a0;
      bf16x8 wf[2][5][2];
      auto rdm = [&](int b) {
#pragma unroll
        for (int n = 0; n < 5; ++n) {
          wf[b & 1][n][0] = cat8(trread(wm0 + (5 * b + n) * 2048), trread(wm0 + (5 * b + n) * 2048 + 256));
          wf[b & 1][n][1] = cat8(trread(wm1 + (5 * b + n) * 2048), trread(wm1 + (5 * b + n) * 2048 + 256));
        }
        SB();
      };
      rdm(0);
      __builtin_amdgcn_s_setprio(MPRIO);
#pragma unroll
      for (int b = 0; b < 4; ++b) {
        if (b < 3) rdm(b + 1);
#pragma unroll
        for (int n = 0; n < 5; ++n) {
          a0 = __builtin_amdgcn_mfma_f32_16x16x32_bf16(wf[b & 1][n][0], xy[(5 * b + n) >> 2][(5 * b + n) & 3], a0, 0, 0, 0);
          a1 = __builtin_amdgcn_mfma_f32_16x16x32_bf16(wf[b & 1][n][1], xy[(5 * b + n) >> 2][(5 * b + n) & 3], a1, 0, 0, 0);
        }
        SB();
      }
      __builtin_amdgcn_s_setprio(3);
      *lds<u32x2>(qa0) = (u32x2){pk2(a0[0], a0[1]), pk2(a0[2], a0[3])};
      *lds<u32x2>(qa1) = (u32x2){pk2(a1[0], a1[1]), pk2(a1[2], a1[3])};
    }
    if (wq4 < 3) {
#pragma unroll
      for (int i = 0; i < 4; ++i) *lds<u32x4>(img + 16640 + lane * 16 + i * 1024) = t[i];
    }
    lds_arrive(CNT(C_CS));
    lds_wait<2>(CNT(C_CS), 4);
    STAMP(5);
    put_o((u32x4){~0u, ~0u, ~0u, ~0u}, (u32x2){~0u, ~0u});
    tile64(L_QT, Aq);
    if (g & 1) conv_shared<0x3B, 6>(L_CV, wq4, lane);
    else conv_shared<0xC4, 8>(L_CV, wq4, lane);
    lds_arrive(CNT(C_CR));
    lds_wait<1>(CNT(C_CR), 4);
    if (wq4 < 3) {
      const u32 n = 2 * wq4 + h;
      const u32 tile = (((g & 1) ? 0x54310u : 0x762u) >> (4 * n)) & 7;
      f32x4 p[8];
#pragma unroll
      for (int i = 0; i < 8; ++i) p[i] = *lds<f32x4>(L_CRED + (i >> 1) * 5120 + (i & 1) * 16 + wq4 * 2048 + lane * 32);
      SB();
      const f32x4 a = add4(add4(p[0], p[2]), add4(p[4], p[6])), b = add4(add4(p[1], p[3]), add4(p[5], p[7]));
      __builtin_amdgcn_raw_buffer_store_b128((u32x4){pk2s(a[0], a[1]), pk2s(a[2], a[3]), pk2s(b[0], b[1]), pk2s(b[2], b[3])}, rsW,
                                             n < ((g & 1) ? 5u : 3u) ? WS_YT + hq * HS + (tile) * 40960 + chan(0) * 512 + l31 * 16 : OOB, 0, SC1);
    }
    STAMP(6);
    STAMP(7); CSTAMP(30);
    LOAD_A();
    read_q();
    zero_o();
    vmw<8>();
    HOLD(5, 3000);
    lds_arrive(CNT(C_CH));
    __builtin_amdgcn_s_setprio(1);
  } else {
    f32x4 acc[3];
    u32 qa[3];
#pragma unroll
    for (int i = 0; i < 3; ++i) {
      acc[i] = (f32x4){0.f, 0.f, 0.f, 0.f};
      qa[i] = qaddr(2 * hp + i);
    }
    const u32 wl = L_RING + (16 * (gq >> 1) + 4 * (gq & 1) + ((lane >> 2) & 3)) * 96 + 8 * (lane & 3);
    bf16x8 xf[3][4], wf[2][4][3];
    u64 fl = 0;
    zero_o();
#pragma unroll
    for (int s = 0; s < 5; ++s) {
      if (s == 0) vmw<8>();
      if (s == 1) vm0();
      if (s == 0) STAMP(15);
      bar();
      if (s == 0) STAMP(16);
      if (s == 1) STAMP(18);
      if (s == 4) STAMP(19);
      if (s == 3) fl = __hip_atomic_load((GAS u64*)kvflag, __ATOMIC_RELAXED, __HIP_MEMORY_SCOPE_AGENT);
      const u32 xo = mod5(rot + s + 1) * 16896;
      auto rdw = [&](int ks) {
#pragma unroll
        for (int i = 0; i < 3; ++i) {
          const u32 wb = wl + s * 12288 + ks * 3072 + i * 32;
          wf[s & 1][ks][i] = cat8(trread(wb), trread(wb + 768));
        }
        if (s == 0) xf[0][ks] = *lds<bf16x8>(xa[ks] + rot * 16896);
        if (s < 4) xf[(s + 1) % 3][ks] = *lds<bf16x8>(xa[ks] + xo);
      };
      auto mma = [&](int st, int ks) {
#pragma unroll
        for (int i = 0; i < 3; ++i) acc[i] = __builtin_amdgcn_mfma_f32_16x16x32_bf16(wf[st & 1][ks][i], xf[st % 3][ks], acc[i], 0, 0, 0);
      };
#pragma unroll
      for (int ks = 0; ks < 4; ++ks) {
        rdw(ks);
        SB();
        if (s > 0) mma(s - 1, ks);
        SB();
      }
      if (s == 3) {
        if (!__all(fl == tag)) wait_flags(kvflag, true, tag);
        asm volatile("" ::: "memory");
        STAMP(7); CSTAMP(30);
        LOAD_A();
      }
      if (s == 4) {
#pragma unroll
        for (int ks = 0; ks < 4; ++ks) mma(4, ks);
      }
    }
    STAMP(3);
#pragma unroll
    for (int i = 0; i < 3; ++i) *lds<u32x2>(qa[i]) = (u32x2){pk2(acc[i][0], acc[i][1]), pk2(acc[i][2], acc[i][3])};
    bar();
    read_q();
    __builtin_amdgcn_s_setprio(2);
    STAMP(4);
  }
  u32x4 yv[4];
  auto yload = [&]() {
#pragma unroll
    for (int i = 0; i < 4; ++i)
      yv[i] = __builtin_amdgcn_raw_buffer_load_b128(rsW, (lane >> 3) * 512 + (lane & 7) * 16,
                                                    wq4 * 4 + i < 10 ? WS_YT + hq * HS + ((g >> 2)) * 40960 + (g & 3) * 128 + (wq4 * 4 + i) * 4096 : OOB, SC1);
    SB();
  };
  {
    const int idx = (int)0xDDDDDDDDu;
    const u32 cntA = half ? CNT(C_AT1) : CNT(C_AT0);
    asm volatile("s_waitcnt vmcnt(0)" : "+v"(A.k1a), "+v"(A.k1b), "+v"(A.v0), "+v"(A.v1), "+v"(A.v2), "+v"(A.v3), "+v"(A.k2a), "+v"(A.k2b)::"memory");
    f32x16 Sx[4];
    Sx[0] = qk8(A.k2a, q2[0], qk16(A.k1a, q1[0]));
    SB();
    Sx[1] = qk8(A.k2a, q2[1], qk16(A.k1a, q1[1]));
    SB();
    LOAD_REC(B, rec0 + 7168);
    STAMP(28);
    __builtin_amdgcn_s_waitcnt(0xC07F);
    SB();
    REC_STEPS(A, B, 0); REC_STEPS(B, A, 1); REC_STEPS(A, B, 2); REC_STEPS(B, A, 3);
    REC_STEPS(A, B, 4); REC_STEPS(B, A, 5); REC_STEPS(A, B, 6);
    const u32 ored = half ? L_OY : L_OX;
    u32 ga;
    auto give = [&](int i, int j) {
      if (j == 0) { ga = ored + (((wq4 + 1 + i) & 3) * 3 + i) * 3072 + lane * 16; asm volatile("" : "+v"(ga)); }
      *lds<f32x4>(ga + j * 1024) = (f32x4){O[i][4 * j], O[i][4 * j + 1], O[i][4 * j + 2], O[i][4 * j + 3]};
    };
    u32 cdv;
    const u32 cfree = half ? CNT(C_CD) : CNT(C_CS);
    STEPX(0, 0, 3, 1, A.v3, B.v0, B.k1a, B.k2a, 1, , cdv = *(volatile LAS u32*)(size_t)cfree);
    STEPX(1, 0, 0, 1, B.v1, B.v2, B.k1b, B.k2b, 1, ,
          if (__builtin_expect(__builtin_amdgcn_readfirstlane(cdv) < 4, 0)) lds_wait<1>(cfree, 4));
    STEPX(2, 1, 0, 2, B.v3, B.v0, B.k1a, B.k2a, 1, , give(0, 0));
    STEPX(3, 1, 1, 2, B.v1, B.v2, B.k1b, B.k2b, 1, give(0, 1), give(0, 2));
    STEPX(0, 2, 1, 3, B.v3, B.v0, B.k1a, B.k2a, 1, , give(1, 0));
    STEPX(1, 2, 2, 3, B.v1, B.v2, B.k1b, B.k2b, 1, give(1, 1), give(1, 2));
    STEPX(2, 3, 2, 0, B.v3, B.v0, B.k1a, B.k2a, 0, , give(2, 0); give(2, 1); give(2, 2));
    EXP4(Sx[3], 0); SB();
    O[3] = pv_hi(B.v1, Sx[2], O[3], idx); SB();
    EXP4(Sx[3], 4); SB();
    EXP4(Sx[3], 8); SB();
    O[3] = pv_lo(B.v2, Sx[3], O[3], idx); SB();
    lds_arrive(cntA);
    EXP4(Sx[3], 12); SB();
    O[3] = pv_hi(B.v3, Sx[3], O[3], idx); SB();
    STAMP(8); CSTAMP(31);
    lds_wait(cntA, 4);
    STAMP(21);
    f32x4 pr[9], t[3];
#pragma unroll
    for (int i = 0; i < 9; ++i) pr[i] = *lds<f32x4>(ored + wq4 * 9216 + lane * 16 + (8 - i) * 1024);
    SB();
    if (half == 1) yload();
#pragma unroll
    for (int i = 0; i < 3; ++i) t[i] = add4(add4((f32x4){O[3][4 * i], O[3][4 * i + 1], O[3][4 * i + 2], O[3][4 * i + 3]}, pr[2 - i]), add4(pr[5 - i], pr[8 - i]));
    float lsum = upper_half(t[2][0]);
    if (__builtin_expect(!__all(lsum >= 5.421010862427522e-20f && lsum <= 1.8446744073709552e19f), 0)) {
      float m = -INFINITY;
#pragma unroll 1
      for (int r = 0; r < 64; ++r) {
        const u32 so = WS_REC + (r >> 1) * 7168;
        const f32x16 sa = qk8(LD64(2048, so + (r & 1) * 512), q2[3], qk16(LD128(0, so + (r & 1) * 1024), q1[3]));
#pragma unroll
        for (int i = 0; i < 16; ++i) m = fmaxf(m, sa[i]);
      }
      const auto mm = __builtin_amdgcn_permlane32_swap(__builtin_bit_cast(u32, m), __builtin_bit_cast(u32, m), false, false);
      const float M = -fmaxf(asf(mm[0]), asf(mm[1]));
      const u32 m0 = __builtin_bit_cast(u32, M) & 0xffff0000u;
      const float r1 = M - __builtin_bit_cast(float, m0);
      const u32 m1 = __builtin_bit_cast(u32, r1) & 0xffff0000u;
      const float r2 = r1 - __builtin_bit_cast(float, m1);
      const u32x2 qs = h ? (u32x2){(m0 >> 16) | m1, __builtin_bit_cast(u32, r2) >> 16} : q2[3];
      f32x16 o;
#pragma unroll
      for (int i = 0; i < 16; ++i) o[i] = 0.f;
#pragma unroll 1
      for (int r = 0; r < 64; ++r) {
        const u32 so = WS_REC + (r >> 1) * 7168;
        f32x16 p = qk8(LD64(2048, so + (r & 1) * 512), qs, qk16(LD128(0, so + (r & 1) * 1024), q1[3]));
        const u32x4 va = LDV(0, so + 3072 + (r & 1) * 2048), vb = LDV(1024, so + 3072 + (r & 1) * 2048);
        EXP4(p, 0); EXP4(p, 4); EXP4(p, 8); EXP4(p, 12);
        o = pv_hi(vb, p, pv_lo(va, p, o, idx), idx);
      }
#pragma unroll
      for (int i = 0; i < 3; ++i) t[i] = (f32x4){o[4 * i], o[4 * i + 1], o[4 * i + 2], o[4 * i + 3]};
      lsum = upper_half(o[8]);
    }
    if (half == 1) HOLD(4, 4500);
    float inv = __builtin_amdgcn_rcpf(lsum);
    asm volatile("s_nop 0" : "+v"(inv));
    auto fin = [&](float a, float b) { return pk2s(mul1(a, inv), mul1(b, inv)); };
    put_o((u32x4){fin(t[0][0], t[0][1]), fin(t[0][2], t[0][3]), fin(t[1][0], t[1][1]), fin(t[1][2], t[1][3])},
          (u32x2){fin(t[2][0], t[2][1]), fin(t[2][2], t[2][3])});
    STAMP(22);
  }
  char* const oflag = ws + WS_FO + ((g * 8 + hq) * 4 + wq4) * FSO;
  const rsrc_t rsOut = mkrs(out, S * 2 * D * 2);
  if (half == 1) {
    __builtin_amdgcn_s_sleep(PSLEEP);
    lds_arrive(CNT(C_PUB));
    STAMP(9);
    if (wq4 < 3) {
#pragma unroll 1
      for (u32 spins = 0; spins < SPIN_CAP; ++spins) {
        u32 mx = 0;
#pragma unroll
        for (int i = 0; i < 4; ++i) mx = umax_(umax_(umax_(umax_(mx, yv[i][0]), yv[i][1]), yv[i][2]), yv[i][3]);
        if (__all(mx != ~0u)) break;
        lds_wait<1>(CNT(C_A3), 8);
        yload();
      }
      lds_wait<1>(CNT(C_GO), 3);
      STAMP(23);
      const u32 scr = L_GO + wq4 * 8192 + 4096;
      u32x4 qq[4], md[4];
#pragma unroll
      for (int i = 0; i < 4; ++i) {
        qq[i] = *lds<u32x4>(scr - 4096 + i * 1024 + lane * 16);
        md[i] = *lds<u32x4>(scr + (wq4 < 2 ? i : (i & 1)) * 1024 + lane * 16);
      }
      lgkm0();
#pragma unroll
      for (int i = 0; i < 4; ++i) {
        u32x4 y2;
#pragma unroll
        for (int j = 0; j < 4; ++j) y2[j] = pk2(add1(lo2f(yv[i][j]), lo2f(md[i][j])), add1(hi2f(yv[i][j]), hi2f(md[i][j])));
        const u32 chl = 8 * i + (lane >> 3);
        if (wq4 < 2 || i < 2) *lds<u32x4>(scr + chl * 128 + ((lane & 7) ^ (chl >> 2)) * 16) = y2;
      }
      lgkm0();
      const u32 run = lane >> 3, cq = wq4 * 8 + (lane & 7);
      const u32 ob = cq < 20 ? (64 * g + 8 * run) * 2560 + hq * 160 + cq * 8 : OOB;
      u32x4 z[4];
      u32x2 res[8];
#pragma unroll
      for (int c = 0; c < 4; ++c) z[c] = *lds<u32x4>(scr + (((lane & 7) * 4 + c) & (wq4 < 2 ? 31 : 15)) * 128 + (run ^ (lane & 7)) * 16);
#pragma unroll
      for (int i = 0; i < 8; ++i) {
        float r[4];
#pragma unroll
        for (int c = 0; c < 4; ++c) {
          const u32 qv = qq[i >> 1][2 * (i & 1) + (c >> 1)];
          r[c] = mul1((i & 1) ? hi2f(z[c][i >> 1]) : lo2f(z[c][i >> 1]), (c & 1) ? hi2f(qv) : lo2f(qv));
        }
        res[i] = (u32x2){pk2(r[0], r[1]), pk2(r[2], r[3])};
      }
      lds_wait<1>(CNT(C_A3), 8);
#pragma unroll
      for (int i = 0; i < 8; ++i) __builtin_amdgcn_raw_buffer_store_b64(res[i], rsOut, ob, i * 2560, A3AUX);
    }
  } else {
    if (blockIdx.x == 0 && threadIdx.x == 0) put_flag(ws + WS_E, tag);
    lds_wait<2>(CNT(C_CH), 4);
    u32x4 kvv[4];
    u32x2 qr[8];
#pragma unroll
    for (int i = 0; i < 8; ++i) qr[i] = *lds<u32x2>(L_QT + (8 * (lane >> 3) + i) * 160 + (w * 8 + (lane & 7)) * 8);
#pragma unroll
    for (int i = 0; i < 4; ++i) {
      const u32 chl = 8 * i + (lane >> 3);
      kvv[i] = *lds<u32x4>(L_A1 + w * 4096 + chl * 128 + ((7 - (lane & 7)) ^ (chl & 7)) * 16);
    }
    SB();
    const rsrc_t rsO = mkrs(Bwo, D * D * 2);
    u32 kp2[2], jo2[2];
#pragma unroll
    for (int i = 0; i < 2; ++i) {
      const u32 c = (i ? 256 : 64 * wq4) + lane, pos = c / 10;
      kp2[i] = swap23(pos);
      jo2[i] = (c - pos * 10) * 16;
    }
    const u32 vm0_ = (kp2[0] + (kp2[0] >> 4) * 4) * 1280 + jo2[0], vm1_ = (kp2[1] + 4) * 1280 + jo2[1];
    auto trow = [&](u32 kp) { return (kp >> 3) * 80 + ((kp >> 2) & 1) * 20 + 16 + (kp & 3); };
    const u32 vt0 = (trow(kp2[0]) + (kp2[0] >> 4) * 40) * 1280 + jo2[0], vt2 = (trow(kp2[0]) + 40 - (kp2[0] >> 4) * 40) * 1280 + jo2[0];
    const u32 vt1 = trow(kp2[1]) * 1280 + jo2[1];
    auto oload = [&](u32x4 (&xf)[10], int kh) {
#pragma unroll
      for (int ks = 0; ks < 10; ++ks)
        xf[ks] = __builtin_amdgcn_raw_buffer_load_b128(rsW, lane * 16 + (ks & 3) * 1024, WS_O + g * GS + (w * 20 + 10 * kh + (ks & ~3)) * 1024, SC1);
      SB();
    };
    u32x4 xe[10];
    {
      const u32 img = L_CV + (1 + (wq4 >> 1)) * CV_CH;
      const u32 xb = conv_xb(img, lane) + (wq4 & 1) * 256, hb = conv_hb(img, lane) + (wq4 & 1) * 256;
      f32x4 acc[8];
#pragma unroll
      for (int i = 0; i < 8; ++i) acc[i] = (f32x4){0.f, 0.f, 0.f, 0.f};
      auto wo_dma = [&](int n) {
        const int q = (n / 5 + g) & 3, e = q >> 1, c = 4 * (q & 1) + (n % 5 < 4 ? n % 5 : wq4);
        if (n < 20) dma16(L_W + (10 * e + c) * 5120 + (n % 5 < 4 ? wq4 : 4) * 1024, n % 5 < 4 ? vm0_ : vm1_, rsO, (80 * c + 40 * (((c >> 1) & 1) ^ e)) * 1280 + hq * 160);
        else if (n < 24) dma16(L_W + (10 * ((n >> 1) & 1) + 8 + (n & 1)) * 5120 + wq4 * 1024, (n & 2) ? vt2 : vt0, rsO, (n & 1) * 320 * 1280 + hq * 160);
        else dma16(L_W + (10 * (wq4 >> 1) + 8 + (wq4 & 1)) * 5120 + 4096, vt1, rsO, ((wq4 & 1) * 320 + 40 * (1 - (wq4 >> 1))) * 1280 + hq * 160);
      };
      auto residue = [&](auto BB, const CFrag<8>& f, CFrag<8>& n) {
        constexpr int B = decltype(BB)::value;
        static_for<36>([&](auto NN) {
          constexpr int N = decltype(NN)::value, a = tri_a(N), c = 7 - a - (N - tri_0(a));
          acc[a + c] = __builtin_amdgcn_mfma_f32_16x16x32_bf16(__builtin_bit_cast(bf16x8, f.a[a]), __builtin_bit_cast(bf16x8, f.b[c]), acc[a + c], 0, 0, 0);
          if constexpr (B < 3 && N < 8) n.b[7 - N] = *lds<u32x4>(xb + 64 * (B + 1) + 512 * N);
          if constexpr (B < 3 && N >= 8 && N < 24) {
            const u32x2 p = *lds<u32x2>(hb + 64 * (B + 1) + 512 * ((N - 8) >> 1) + 8 * (N & 1));
            n.a[(N - 8) >> 1][2 * (N & 1)] = p[0];
            n.a[(N - 8) >> 1][2 * (N & 1) + 1] = p[1];
          }
          if constexpr (B > 0 && N % 4 == 0 && (B - 1) * 9 + N / 4 < 25) wo_dma((B - 1) * 9 + N / 4);
          SB();
        });
      };
      lds_arrive(CNT(C_OR));
      CFrag<8> f0, f1;
      conv_load(f0, hb, xb);
      SB();
      STAMP(5);
      __builtin_amdgcn_s_setprio(CP0);
      residue(IC<0>{}, f0, f1);
      __builtin_amdgcn_s_setprio(2);
      vm0();
      if (lane == 0) put_flag(oflag, tag);
      lds_wait<1>(CNT(C_OR), 4);
      const char* eflag = ws + WS_FO + ((g * 8 + (l15 >> 1)) * 4 + (w & 2) + (l15 & 1)) * FSO;
      asm volatile("" : "+v"(eflag));
      __builtin_amdgcn_s_setprio(CP1);
      residue(IC<1>{}, f1, f0);
      __builtin_amdgcn_s_setprio(CP2);
      residue(IC<2>{}, f0, f1);
      __builtin_amdgcn_s_setprio(CP3);
      residue(IC<3>{}, f1, f0);
      __builtin_amdgcn_s_setprio(2);
      const u64 ef = __hip_atomic_load((GAS u64*)eflag, __ATOMIC_RELAXED, __HIP_MEMORY_SCOPE_AGENT);
      SB();
      const u32 xch = L_CV + 4096;
      if (wq4 & 1) {
#pragma unroll
        for (int i = 0; i < 4; ++i) { *lds<f32x4>(xch + wq4 * 4096 + i * 1024 + lane * 16) = acc[i]; acc[i] = acc[4 + i]; }
      } else {
#pragma unroll
        for (int i = 0; i < 4; ++i) *lds<f32x4>(xch + wq4 * 4096 + i * 1024 + lane * 16) = acc[4 + i];
      }
      lds_arrive(CNT(C_P0) + (wq4 >> 1) * 4);
      lds_wait<1>(CNT(C_P0) + (wq4 >> 1) * 4, 2);
      const u32 stg = img + (wq4 & 1) * 2048;
#pragma unroll
      for (int i = 0; i < 4; ++i) {
        const f32x4 v = add4(acc[i], *lds<f32x4>(xch + (wq4 ^ 1) * 4096 + i * 1024 + lane * 16));
        *lds<u32x2>(stg + 512 * i + 32 * l15 + 8 * gq) = (u32x2){pk2s(v[0], v[1]), pk2s(v[2], v[3])};
      }
      lgkm0();
      const u32 yo = WS_YT + hq * HS + ((wq4 & 1) * 4 + h) * 40960 + chan(1 + (wq4 >> 1)) * 512 + l31 * 16;
      __builtin_amdgcn_raw_buffer_store_b128(*lds<u32x4>(stg + lane * 16), rsW, yo, 0, SC1);
      __builtin_amdgcn_raw_buffer_store_b128(*lds<u32x4>(stg + 1024 + lane * 16), rsW, yo + 81920, 0, SC1);
      vmw<2>();
      lds_arrive(CNT(C_CD));
      if (!__all(ef == tag)) wait_flags<2>(eflag, true, tag);
      asm volatile("" ::: "memory");
      oload(xe, 0);
    }
    STAMP(6);
    lds_wait<1>(CNT(C_CD), 4);
    if (w < 3) {
#pragma unroll
      for (int i = 0; i < 4; ++i) {
        *lds<u32x4>(L_GO + w * 8192 + i * 1024 + lane * 16) = (u32x4){qr[2 * i][0], qr[2 * i][1], qr[2 * i + 1][0], qr[2 * i + 1][1]};
        if (w < 2 || i < 2) *lds<u32x4>(L_GO + w * 8192 + 4096 + i * 1024 + lane * 16) = kvv[i];
      }
      lds_arrive(CNT(C_GO));
    }
    f32x4 acc[5];
#pragma unroll
    for (int i = 0; i < 5; ++i) acc[i] = (f32x4){0.f, 0.f, 0.f, 0.f};
    const u32 wl = L_W + (16 * (gq >> 1) + 4 * (gq & 1) + ((lane >> 2) & 3)) * 160 + 8 * (lane & 3);
    auto wfrag = [&](u32 wb, int ks, int nt) { return cat8(trread(wb + ks * 5120 + nt * 32), trread(wb + ks * 5120 + 1280 + nt * 32)); };
    {
      lds_wait<2>(CNT(C_AT1), EGN);
      STAMP(25);
      __builtin_amdgcn_s_setprio(EPRIO);
      const u32 wb = wl;
      bf16x8 wf[2][5];
#pragma unroll
      for (int nt = 0; nt < 5; ++nt) wf[0][nt] = wfrag(wb, 0, nt);
      SB();
#pragma unroll
      for (int ks = 0; ks < 10; ++ks) {
#pragma unroll
        for (int nt = 0; nt < 5; ++nt) {
          if (ks < 9) wf[(ks + 1) & 1][nt] = wfrag(wb, ks + 1, nt);
          acc[nt] = __builtin_amdgcn_mfma_f32_16x16x32_bf16(wf[ks & 1][nt], __builtin_bit_cast(bf16x8, xe[ks]), acc[nt], 0, 0, 0);
        }
        SB();
      }
      STAMP(26);
    }
    SB();
    {
      const int kh = 1;
      const u32 wb = wl + kh * 51200;
      constexpr int NPRE = 4;
      bf16x8 wf[NPRE][5];
#pragma unroll
      for (int ks = 0; ks < NPRE; ++ks)
#pragma unroll
        for (int nt = 0; nt < 5; ++nt) wf[ks][nt] = wfrag(wb, ks, nt);
      SB();
      STAMP(10);
      lds_arrive(CNT(C_A3));
      STAMP(45);
      __builtin_amdgcn_s_setprio(2);
      lds_wait<1>(CNT(C_A3), 4);
      lds_wait<1>(CNT(C_PUB), 4);
      f32x4 res[5];
#pragma unroll 1
      for (u32 spins = 0; spins < SPIN_CAP; ++spins) {
        u32x4 xf[10];
        asm volatile("" ::: "memory");
        oload(xf, kh);
        bf16x8 wl2[2][5];
#pragma unroll
        for (int ks = 0; ks < 2; ++ks)
#pragma unroll
          for (int nt = 0; nt < 5; ++nt) wl2[ks][nt] = wfrag(wb, NPRE + ks, nt);
        SB();
        u32 mx = 0;
#pragma unroll
        for (int ks = 0; ks < 10; ++ks) {
#pragma unroll
          for (int nt = 0; nt < 5; ++nt) {
            res[nt] = __builtin_amdgcn_mfma_f32_16x16x32_bf16(ks < NPRE ? wf[ks < NPRE ? ks : 0][nt] : wl2[ks & 1][nt], __builtin_bit_cast(bf16x8, xf[ks]),
                                                              ks ? res[nt] : acc[nt], 0, 0, 0);
            if (ks >= NPRE && ks < 8) wl2[ks & 1][nt] = wfrag(wb, ks + 2, nt);
          }
          mx = umax_(umax_(umax_(umax_(mx, xf[ks][0]), xf[ks][1]), xf[ks][2]), xf[ks][3]);
          SB();
        }
        if (__all(mx != ~0u)) break;
      }
      lds_arrive(CNT(C_A3));
      STAMP(11);
#pragma unroll
      for (int nt = 0; nt < 5; ++nt) acc[nt] = res[nt];
      STAMP(27);
    }
    const u32 stg = L_W + w * 4096;
#pragma unroll
    for (int i = 0; i < 5; ++i) *lds<u32x2>(stg + l15 * 160 + i * 32 + gq * 8) = (u32x2){pk2(acc[i][0], acc[i][1]), pk2(acc[i][2], acc[i][3])};
    lgkm0();
    asm volatile(".Lpf_end:");
    const u32 ob = (64 * g + 16 * w) * 2560 + 1280 + hq * 160;
#pragma unroll
    for (int i = 0; i < 3; ++i) {
      const u32 c = i < 2 ? i * 64 + lane : 128 + l31, r = c / 10;
      __builtin_amdgcn_raw_buffer_store_b128(*lds<u32x4>(stg + c * 16), rsOut, (i == 2 && h) ? OOB : ob + r * 2560 + (c - r * 10) * 16, 0, FAUX);
    }
  }
  HOLD(1, 6000);
  STAMP_OUT;
}

extern "C" void kernel_launch(void* const* d_in, const int* in_sizes, int n_in, void* d_out, int out_size, void* d_ws, size_t ws_size,
                              hipStream_t stream) {
  fused_kernel<<<256, 512, 0, stream>>>((const u16*)d_in[6], (const u16*)d_in[8], (const u16*)d_in[9], (const u16*)d_in[7], (char*)d_ws,
                                        (const u16*)d_in[1], (const u16*)d_in[2], (const u16*)d_in[3], (const u16*)d_in[4],
                                        (const u16*)d_in[0], (const u16*)d_in[5], (const u16*)d_in[10], (u16*)d_out);
}
```

```cpp
#include <hip/hip_runtime.h>
#include <hip/hip_bf16.h>

typedef unsigned short u16;
typedef unsigned int u32;
typedef unsigned long long u64;
typedef __bf16 bf16x2 __attribute__((ext_vector_type(2)));
typedef __bf16 bf16x8 __attribute__((ext_vector_type(8)));
typedef __bf16 bf16x16 __attribute__((ext_vector_type(16)));
typedef short s16x4 __attribute__((ext_vector_type(4)));
typedef float f32x2 __attribute__((ext_vector_type(2)));
typedef float f32x4 __attribute__((ext_vector_type(4)));
typedef float f32x8 __attribute__((ext_vector_type(8)));
typedef float f32x16 __attribute__((ext_vector_type(16)));
typedef u32 u32x2 __attribute__((ext_vector_type(2)));
typedef u32 u32x4 __attribute__((ext_vector_type(4)));
typedef __amdgpu_buffer_rsrc_t rsrc_t;
#define LAS __attribute__((address_space(3)))
#define GAS __attribute__((address_space(1)))
#define DI __device__ __forceinline__
#define SC1 16
#define NT 2
#define OOB 0x80000000u
#define SB() __builtin_amdgcn_sched_barrier(0)

constexpr int S = 2048, D = 640;
constexpr int NREP = 8;
constexpr u32 WS_E = 0;
constexpr u32 WS_FKV = 4096;
constexpr u32 FSA = 8, FSO = 8;
constexpr u32 WS_FA1 = WS_FKV + NREP * 2048;
constexpr u32 WS_FO = WS_FA1 + 8192;
constexpr u32 WS_DBG = 65536;
constexpr u32 WS_REC = 131072;
constexpr u32 WS_KVT = 524288;
constexpr u32 HS = 327680 + 128, GS = 81920 + 128;
constexpr u32 WS_YT = WS_KVT + 8 * HS;
constexpr u32 WS_O = WS_YT + 8 * HS;
constexpr u32 WS_END = WS_O + 32 * GS;
constexpr u32 L_X = 0;
constexpr u32 L_RING = 84480;
constexpr u32 L_WK = L_RING + 40960;
constexpr u32 L_PART = L_WK + 25600;
constexpr u32 L_PAR = L_PART + 8192;
constexpr u32 L_QF = 0;
constexpr u32 L_KV = 12288;
constexpr u32 L_A1 = 34816;
constexpr u32 L_OX = 49152;
constexpr u32 L_WM = 45056;
constexpr u32 L_QT = 86016;
constexpr u32 L_CRED = 12288;
constexpr u32 L_W = 0;
constexpr u32 CV_CH = 21248;
constexpr u32 L_CNT = 163840 - 128;
constexpr u32 L_CV = L_CNT - 3 * CV_CH;
constexpr u32 L_OY = 102400;
constexpr u32 L_GO = L_OY + 36864;
constexpr u32 L_TAG = L_CNT + 120;
constexpr u32 L_END = 163840;
static_assert(L_KV + 22080 <= L_A1 && L_GO + 22528 <= L_CNT && L_PART + 8192 <= L_CNT, "lds");
enum { C_PART, C_KT, C_CS, C_CH, C_P0, C_P1, C_CD, C_A3, C_AT0, C_AT1, C_OR, C_PUB, C_GO, C_K0, C_K1, C_WM, C_SW, C_N };
#define CNT(i) (L_CNT + 4 * (i))

#define STAMP(i) do {} while (0)
#define CSTAMP(i) do {} while (0)
#define STAMP_INIT do {} while (0)
#define STAMP_OUT do {} while (0)

#define LSTAMP(i) do {} while (0)
DI rsrc_t mkrs(const void* p, u32 bytes) { return __builtin_amdgcn_make_buffer_rsrc((void*)p, (short)0, (int)bytes, 0x00020000); }
DI u32 pk2(float a, float b) { f32x2 v = {a, b}; return __builtin_bit_cast(u32, __builtin_convertvector(v, bf16x2)); }
DI float lo2f(u32 x) { return __builtin_bit_cast(float, x << 16); }
DI float hi2f(u32 x) { return __builtin_bit_cast(float, x & 0xffff0000u); }
DI float asf(u32 x) { return __builtin_bit_cast(float, x); }
DI u32 asu(float x) { return __builtin_bit_cast(u32, x); }
DI float upper_half(float x) { asm volatile("s_nop 1" : "+v"(x)); const auto r = __builtin_amdgcn_permlane32_swap(asu(x), asu(x), false, false); return asf(r[1]); }
DI u32 umin_(u32 a, u32 b) { return a < b ? a : b; }
DI u32 umax_(u32 a, u32 b) { return a > b ? a : b; }
DI float add1(float a, float b) { float r; asm("v_add_f32 %0, %1, %2" : "=v"(r) : "v"(a), "v"(b)); return r; }
DI float mul1(float a, float b) { float r; asm("v_mul_f32 %0, %1, %2" : "=v"(r) : "v"(a), "v"(b)); return r; }
DI float fma1(float a, float b, float c) { float r; asm("v_fma_f32 %0, %1, %2, %3" : "=v"(r) : "v"(a), "v"(b), "v"(c)); return r; }
DI f32x4 add4(f32x4 a, f32x4 b) { return (f32x4){add1(a[0], b[0]), add1(a[1], b[1]), add1(a[2], b[2]), add1(a[3], b[3])}; }
DI u32 pk2s(float a, float b) { return umin_(pk2(a, b), 0xFFFFFFFEu); }
DI void lgkm0() { asm volatile("s_waitcnt lgkmcnt(0)" ::: "memory"); }
DI void vm0() { asm volatile("s_waitcnt vmcnt(0)" ::: "memory"); }
template <int N> DI void vmw() { asm volatile("s_waitcnt vmcnt(%0)" ::"n"(N) : "memory"); }
DI void bar() { asm volatile("s_waitcnt lgkmcnt(0)\n\ts_barrier" ::: "memory"); }
template <class T> DI LAS T* lds(u32 off) { return (LAS T*)(size_t)off; }
DI s16x4 trread(u32 addr) { return __builtin_bit_cast(s16x4, __builtin_amdgcn_ds_read_tr16_b64_v4i16((LAS s16x4*)(size_t)addr)); }
DI bf16x8 cat8(s16x4 a, s16x4 b) { return __builtin_bit_cast(bf16x8, __builtin_shufflevector(a, b, 0, 1, 2, 3, 4, 5, 6, 7)); }
DI void dma16(u32 lds_dst, u32 voff, rsrc_t rs, u32 soff) {
  u32 keep;
  asm volatile("s_mov_b32 %0, m0\n\ts_mov_b32 m0, %1\n\ts_nop 0\n\tbuffer_load_dwordx4 %2, %3, %4 offen lds\n\ts_mov_b32 m0, %0"
               : "=&s"(keep) : "s"(lds_dst), "v"(voff), "s"(rs), "s"(soff) : "memory");
}
DI void dma16nt(u32 lds_dst, u32 voff, rsrc_t rs, u32 soff) {
  u32 keep;
  asm volatile("s_mov_b32 %0, m0\n\ts_mov_b32 m0, %1\n\ts_nop 0\n\tbuffer_load_dwordx4 %2, %3, %4 offen nt lds\n\ts_mov_b32 m0, %0"
               : "=&s"(keep) : "s"(lds_dst), "v"(voff), "s"(rs), "s"(soff) : "memory");
}
template <int N> struct IC { static constexpr int value = N; };
template <int N, int I = 0, class F> DI void static_for(F f) { if constexpr (I < N) { f(IC<I>{}); static_for<N, I + 1>(f); } }
template <int OFF> DI void d16hi(float& x, u32 addr) { asm volatile("ds_read_u16_d16_hi %0, %1 offset:%2" : "+v"(x) : "v"(addr), "n"(OFF) : "memory"); }
DI u32 swap23(u32 x) { return (x & ~12u) | ((x & 4u) << 1) | ((x & 8u) >> 1); }
DI int mod5(int x) { return x >= 5 ? x - 5 : x; }

constexpr u32 SPIN_CAP = 1u << 17;
DI bool poll_flags(const char* p, bool active, u64 tag) {
  u64 v = tag;
  if (active) v = __hip_atomic_load((GAS u64*)p, __ATOMIC_RELAXED, __HIP_MEMORY_SCOPE_AGENT);
  return __all(v == tag);
}
template <int SLP = 1>
DI void wait_flags(const char* p, bool active, u64 tag) {
  if (__builtin_expect(!poll_flags(p, active, tag), 0)) {
#pragma unroll 1
    for (u32 spins = 0; spins < SPIN_CAP; ++spins) {
      __builtin_amdgcn_s_sleep(SLP);
      if (poll_flags(p, active, tag)) break;
    }
  }
  asm volatile("" ::: "memory");
}
DI void put_flag(char* p, u64 tag) { __hip_atomic_store((GAS u64*)p, tag, __ATOMIC_RELAXED, __HIP_MEMORY_SCOPE_AGENT); }
DI void lds_arrive(u32 cnt) {
  asm volatile("s_waitcnt lgkmcnt(0)\n\ts_mov_b64 exec, 1\n\tds_add_u32 %0, %1\n\ts_mov_b64 exec, -1" ::"v"(cnt), "v"(1u) : "memory");
}
template <int SLP = 0>
DI void lds_wait(u32 cnt, u32 target) {
  u32 v, sv, n = SPIN_CAP << 7;
  asm volatile("1:\n\tds_read_b32 %0, %3\n\ts_sub_u32 %2, %2, 1\n\ts_waitcnt lgkmcnt(0)\n\tv_readfirstlane_b32 %1, %0\n\t"
               "s_cselect_b32 %1, -1, %1\n\ts_cmp_lt_u32 %1, %4\n\ts_cbranch_scc0 2f\n\ts_sleep %5\n\ts_branch 1b\n2:"
               : "=&v"(v), "=&s"(sv), "+s"(n) : "v"(cnt), "s"(target), "n"(SLP) : "memory", "scc");
}

#define CP0 0
#define CP1 0
#define CP2 0
#define CP3 2
#define A3AUX 0
#define EPRIO 0
#define ASLEEP 8
#define PSLEEP 12
#define FAUX 0
#define SPRIO 3
#define XDMA dma16nt
#define FULLKV
#define NOTLB
#define PST 4
#define NE 1
#define ADELAY 0
#define EGN 4
#define HAUX 2
#define YMPOS 0
#define YMAUX 16
#define MPRIO 3
#define KSLEEP 2
#define PUBPOS 1
#define HOLDC C_SW
#define HOLDN 4
#define KLATE 1
#define VSLEEP 3
template <int NA> struct CFrag { u32x4 a[NA], b[NA]; };
template <int NA> DI void conv_load(CFrag<NA>& f, u32 hb, u32 xb) {
#pragma unroll
  for (int a = 0; a < NA; ++a) {
    f.b[NA - 1 - a] = *(volatile LAS u32x4*)(size_t)(xb + 512 * (8 - NA + a));
    const u32x2 p = *(volatile LAS u32x2*)(size_t)(hb + 512 * a), q = *(volatile LAS u32x2*)(size_t)(hb + 512 * a + 8);
    f.a[a] = (u32x4){p[0], p[1], q[0], q[1]};
  }
}
template <int MASK, int NA> DI void conv_mma(const CFrag<NA>& f, f32x4 (&acc)[8]) {
#pragma unroll
  for (int a = 0; a < NA; ++a)
#pragma unroll
    for (int c = NA - 1; c >= 0; --c)
      if (a + c < 8 && ((MASK >> (a + c)) & 1))
        acc[a + c] = __builtin_amdgcn_mfma_f32_16x16x32_bf16(__builtin_bit_cast(bf16x8, f.a[a]), __builtin_bit_cast(bf16x8, f.b[c]), acc[a + c], 0, 0, 0);
}
template <int NM, int ND> DI void weave() {
#pragma unroll
  for (int i = 0; i < NM; ++i) {
    __builtin_amdgcn_sched_group_barrier(0x8, 1, 0);
    __builtin_amdgcn_sched_group_barrier(0x100, ND, 0);
  }
  SB();
}
constexpr int tri_0(int a) { return a * 8 - a * (a - 1) / 2; }
constexpr int tri_a(int n) { int a = 0; while (tri_0(a + 1) <= n) ++a; return a; }
DI u32 conv_xb(u32 img, int lane) { return img + 16640 + 480 - 32 * (lane & 15) + 16 * (lane >> 4); }
DI u32 conv_hb(u32 img, int lane) { const u32 e = (lane & 15) + 1 + 8 * (lane >> 4); return img + (e & 3) * 4160 + (e & ~3u) * 2; }
template <int MASK, int NA>
DI void conv_shared(u32 img, int wq4, int lane) {
  const u32 xb = conv_xb(img, lane) + wq4 * 128, hb = conv_hb(img, lane) + wq4 * 128;
  const u32 dst = L_CRED + wq4 * 5120 + (lane & 15) * 64 + (lane >> 4) * 16;
  f32x4 acc[8];
#pragma unroll
  for (int i = 0; i < 8; ++i) acc[i] = (f32x4){0.f, 0.f, 0.f, 0.f};
  CFrag<NA> f0, f1;
  conv_load(f0, hb, xb);
  SB();
  __builtin_amdgcn_s_setprio(SPRIO);
  conv_load(f1, hb + 64, xb + 64);
  conv_mma<MASK>(f0, acc);
  weave<12, 2>();
  conv_mma<MASK>(f1, acc);
  SB();
  __builtin_amdgcn_s_setprio(3);
  int n = 0;
#pragma unroll
  for (int i = 0; i < 8; ++i)
    if ((MASK >> i) & 1) *lds<f32x4>(dst + 1024 * n++) = acc[i];
}

struct Rec { u32x4 k1a, k1b, v0, v1, v2, v3; u32x2 k2a, k2b; };
#define LD128(off, so) __builtin_amdgcn_raw_buffer_load_b128(rsW, v16 + (off), so, SC1)
#define LDV(off, so) __builtin_amdgcn_raw_buffer_load_b128(rsW, vv + (off), so, SC1)
#define LD64(off, so) __builtin_amdgcn_raw_buffer_load_b64(rsW, v8 + (off), so, SC1)
#define LOAD_H1(R, so) asm volatile( \
    "buffer_load_dwordx4 %0, %4, %7, %8 offen sc1\n\tbuffer_load_dwordx2 %1, %5, %7, %8 offen offset:2048 sc1\n\t" \
    "buffer_load_dwordx4 %2, %6, %7, %8 offen offset:3072 sc1\n\tbuffer_load_dwordx4 %3, %6, %7, %9 offen sc1" \
    : "=&v"(R.k1a), "=&v"(R.k2a), "=&v"(R.v0), "=&v"(R.v1) : "v"(v16), "v"(v8), "v"(vv), "s"(rsW), "s"(so), "s"((so) + 4096) : "memory")
#define LOAD_H2(R, so) asm volatile( \
    "buffer_load_dwordx4 %0, %4, %7, %8 offen offset:1024 sc1\n\tbuffer_load_dwordx2 %1, %5, %7, %8 offen offset:2560 sc1\n\t" \
    "buffer_load_dwordx4 %2, %6, %7, %9 offen offset:1024 sc1\n\tbuffer_load_dwordx4 %3, %6, %7, %9 offen offset:2048 sc1" \
    : "=&v"(R.k1b), "=&v"(R.k2b), "=&v"(R.v2), "=&v"(R.v3) : "v"(v16), "v"(v8), "v"(vv), "s"(rsW), "s"(so), "s"((so) + 4096) : "memory")
#define WAIT_H1(R, n) asm volatile("s_waitcnt vmcnt(" #n ")" : "+v"(R.k1a), "+v"(R.k2a), "+v"(R.v0), "+v"(R.v1)::"memory")
#define WAIT_H2(R, n) asm volatile("s_waitcnt vmcnt(" #n ")" : "+v"(R.k1b), "+v"(R.k2b), "+v"(R.v2), "+v"(R.v3)::"memory")
DI f32x16 qk16(u32x4 k1, u32x4 q1) {
  f32x16 z;
#pragma unroll
  for (int i = 0; i < 16; ++i) z[i] = 0.f;
  return __builtin_amdgcn_mfma_f32_32x32x16_bf16(__builtin_bit_cast(bf16x8, k1), __builtin_bit_cast(bf16x8, q1), z, 0, 0, 0);
}
DI f32x16 qk8(u32x2 k2, u32x2 q2, f32x16 s) {
  return __builtin_amdgcn_mfma_f32_32x32x8bf16_1k(__builtin_bit_cast(s16x4, k2), __builtin_bit_cast(s16x4, q2), s, 0, 0, 0);
}
DI f32x16 pv_lo(u32x4 v, f32x16 p, f32x16 o, int idx) {
  f32x8 h = __builtin_shufflevector(p, p, 0, 1, 2, 3, 4, 5, 6, 7);
  return __builtin_amdgcn_smfmac_f32_32x32x32_bf16(__builtin_bit_cast(bf16x8, v), __builtin_bit_cast(bf16x16, h), o, idx, 0, 0);
}
DI f32x16 pv_hi(u32x4 v, f32x16 p, f32x16 o, int idx) {
  f32x8 h = __builtin_shufflevector(p, p, 8, 9, 10, 11, 12, 13, 14, 15);
  return __builtin_amdgcn_smfmac_f32_32x32x32_bf16(__builtin_bit_cast(bf16x8, v), __builtin_bit_cast(bf16x16, h), o, idx, 0, 0);
}
#define EXP4(X, i) do { X[i] = __builtin_amdgcn_exp2f(X[i]); X[i + 1] = __builtin_amdgcn_exp2f(X[i + 1]); X[i + 2] = __builtin_amdgcn_exp2f(X[i + 2]); X[i + 3] = __builtin_amdgcn_exp2f(X[i + 3]); } while (0)
#define VMWAIT(n) __builtin_amdgcn_s_waitcnt(0x0F70 | ((n) & 15) | (((n) >> 4) << 14))
#define STEPX(SI, H, HP, HN, VPREV, VCUR, K1N, K2N, QK, MID, END) do { \
    EXP4(Sx[SI], 0); SB(); \
    if ((QK) != 2) O[HP] = pv_hi(VPREV, Sx[(SI + 3) & 3], O[HP], idx); \
    SB(); MID; SB(); \
    EXP4(Sx[SI], 4); SB(); \
    if (QK) Sx[(SI + 2) & 3] = qk16(K1N, q1[HN]); \
    SB(); \
    EXP4(Sx[SI], 8); SB(); \
    if (QK) Sx[(SI + 2) & 3] = qk8(K2N, q2[HN], Sx[(SI + 2) & 3]); \
    SB(); \
    EXP4(Sx[SI], 12); SB(); \
    O[H] = pv_lo(VCUR, Sx[SI], O[H], idx); SB(); \
    END; SB(); \
  } while (0)
#define STEP(H, VPREV, VCUR, K1N, K2N, QK, WAITN, REFILL) \
    STEPX(H, H, (H + 3) & 3, (H + 2) & 3, VPREV, VCUR, K1N, K2N, QK, , if ((WAITN) >= 0) VMWAIT((WAITN) >= 0 ? (WAITN) : 0); REFILL)
#define WN(r, i) ((r) < 6 ? 10 : 10 - (i))
#define REC_STEPS(R, N, r) do { \
    const u32 sn = rec0 + ((r) + 1) * 7168 + 4096, so = sn + 3072, sp = so + 4096; \
    STEP(0, N.v3, R.v0, R.k1a, R.k2a, (r) == 0 ? 2 : 1, (r) == 0 ? -1 : WN(r, 0), if ((r) > 0) N.v3 = LDV(2048, sn)); \
    STEP(1, R.v1, R.v0, R.k1a, R.k2a, 1, (r) < 2 ? -1 : WN(r, 1), if ((r) < 2) WAIT_H2(R, 8); if ((r) < 6) R.k1a = LD128(0, so)); \
    STEP(2, R.v1, R.v0, R.k1b, R.k2b, 1, -1, if ((r) < 6) R.k2a = LD64(2048, so)); \
    STEP(3, R.v1, R.v0, R.k1b, R.k2b, 1, (r) == 0 ? -1 : WN(r, 3), if ((r) < 6) R.v0 = LDV(3072, so)); \
    STEP(0, R.v1, R.v2, R.k1b, R.k2b, 1, (r) == 0 ? -1 : WN(r, 4), if ((r) < 6) R.v1 = LDV(0, sp)); \
    STEP(1, R.v3, R.v2, R.k1b, R.k2b, 1, (r) == 0 ? -1 : WN(r, 5), if ((r) == 0) WAIT_H1(N, 8); if ((r) < 6) R.k1b = LD128(1024, so)); \
    STEP(2, R.v3, R.v2, N.k1a, N.k2a, 1, (r) < 6 ? -1 : 2, if ((r) < 6) R.k2b = LD64(2560, so)); \
    if ((r) < 6) STEP(3, R.v3, R.v2, N.k1a, N.k2a, 1, WN(r, 7), R.v2 = LDV(1024, sp)); \
    else STEPX(3, 3, 2, 0, R.v3, R.v2, N.k1b, N.k2b, 1, , VMWAIT(0)); \
    LSTAMP(32 + (r)); \
  } while (0)

__global__ void __launch_bounds__(512, 2)
fused_kernel(const u16* __restrict__ Bx, const u16* __restrict__ Bwk, const u16* __restrict__ Bwv, const u16* __restrict__ Bwq, char* ws,
             const u16* __restrict__ Ak, const u16* __restrict__ Av, const u16* __restrict__ Ash, const u16* __restrict__ Assm,
             const u16* __restrict__ Aq, const u16* __restrict__ AD, const u16* __restrict__ Bwo, u16* __restrict__ out) {
  __shared__ __attribute__((aligned(16))) char smem[L_END];
  asm volatile("" ::"v"((u32)(size_t)(LAS char*)smem));
  const int lane = threadIdx.x & 63;
  const int w = __builtin_amdgcn_readfirstlane(threadIdx.x >> 6);
  const int bj = blockIdx.x >> 3, g = (blockIdx.x & 7) * 4 + (bj >> 3), hq = bj & 7;
  const int gq = lane >> 4, l15 = lane & 15;
  const int half = w >> 2, wq4 = w & 3, h = lane >> 5, l31 = lane & 31;
  const int hp = (hq >> 1) & 1;
  STAMP_INIT;
  STAMP(0); CSTAMP(29);
  const rsrc_t rsW = mkrs(ws, WS_END), rsX = mkrs(Bx, S * D * 2), rsQ = mkrs(Bwq, D * D * 2);
  const rsrc_t rsK = mkrs((hq & 1) ? Bwv : Bwk, D * 20 * 2);
  asm volatile("s_nop 4" ::"s"(rsW), "s"(rsX), "s"(rsQ), "s"(rsK), "s"(Ak), "s"(Av), "s"(Ash), "s"(Assm), "s"(Aq), "s"(AD), "s"(Bwo), "s"(out));

  u32x4 q1[4];
  u32x2 q2[4];
  u64 tag;
  Rec A, B;
  f32x16 O[4];
  u32 vm0_, vm1_, vt0, vt1, vt2;
  auto zero_o = [&]() {
#pragma unroll
    for (int i = 0; i < 4; ++i) {
#pragma unroll
      for (int j = 0; j < 16; ++j) O[i][j] = 0.f;
      asm volatile("" : "+v"(O[i]));
    }
  };
  const u32 v16 = lane * 16, vv = (h * 32 + umin_(l31, 23)) * 16, v8 = umin_(lane, 32) * 8, rec0 = WS_REC + wq4 * 8 * 7168;
  const u32 r6 = lane / 10, vo6 = r6 * 1280 + (lane - r6 * 10) * 16 + hq * 160;
  auto tile64 = [&](u32 dst, const u16* src) {
    const rsrc_t rs = mkrs(src, S * D * 2);
    const int r0 = 64 * g + 18 * wq4;
    if (lane < 60) {
      dma16nt(dst + wq4 * 2880, vo6, rs, r0 * 1280);
      dma16nt(dst + wq4 * 2880 + 960, (wq4 == 3 && r6 > 3) ? OOB : vo6, rs, (r0 + 6) * 1280);
      if (wq4 < 3) dma16nt(dst + wq4 * 2880 + 1920, vo6, rs, (r0 + 12) * 1280);
    }
  };
  const char* kvflag = ws + WS_FKV + (blockIdx.x & (NREP - 1)) * 2048 + (wq4 * 64 + lane) * 8;
  const int rot = g & 3;
  const int rbk = hq >> 1;
  const u32 xvo = gq * 1280 + ((l15 ^ (4 * gq)) * 16);
  auto wsrc = [&](int ph) {
    const u32 c = 64 * ph + lane, p = c / 6;
    return swap23(p) * 1280 + (c - p * 6) * 16;
  };
  auto dma_w = [&](int s, int blk, int ph, u32 vo) {
    dma16(L_RING + s * 12288 + blk * 3072 + ph * 1024, vo, rsQ, (mod5(rot + s) * 128 + blk * 32) * 1280 + hq * 160 + hp * 64);
  };
  auto dma_x = [&](int s, int idx) {
    const int j = mod5(rot + s), m = (4 * rbk + 4 + idx) & 15;
    dma16(L_X + (j * 16 + m) * 1056, xvo, rsX, (64 * g + 4 * m) * 1280 + 256 * j);
  };
  auto qaddr = [&](int tile) {
    const u32 G = 4 * tile + gq, hd = G / 5, ga = G - 5 * hd, q = 16 * (wq4 & 1) + l15, slot = (hd & 1) + (wq4 & 2);
    return L_QF + ((hd >> 1) ^ hp) * 6144 + (ga < 4 ? slot * 1024 + (q + 32 * (ga & 1)) * 16 + (ga >> 1) * 8 : 4096 + slot * 512 + q * 8);
  };
  auto read_q = [&]() {
#pragma unroll
    for (int i = 0; i < 4; ++i) {
      const u32 sl = (wq4 + 1 + i) & 3;
      q1[i] = *lds<u32x4>(L_QF + half * 6144 + sl * 1024 + lane * 16);
      q2[i] = *lds<u32x2>(L_QF + half * 6144 + 4096 + sl * 512 + lane * 8);
    }
  };
  u32 xa[4], wv0, wv1, wv2;
#pragma unroll
  for (int i = 0; i < 4; ++i) xa[i] = L_X + (4 * wq4 + (l15 >> 2)) * 1056 + (l15 & 3) * 256 + gq * 16 + ((i ^ (l15 & 3)) * 64);
  {
    if (w == 0) {
      if (lane < C_N) *lds<u32>(CNT(lane)) = 0;
      lgkm0();
    }
    if (w >= 3) {
      const int j = w - 3;
#pragma unroll
      for (int m = 0; m < 4; ++m) XDMA(L_X + (j * 16 + 4 * rbk + m) * 1056, xvo, rsX, (64 * g + 16 * rbk + 4 * m) * 1280 + 256 * j);
      const int i0 = (bj >> 1) % 5;
#pragma unroll
      for (int i = 0; i < 5; ++i) { const u32 o = j * 5120 + mod5(i0 + i) * 1024; dma16(L_WK + o, lane * 16, rsK, o); }
    }
    else {
      if (w == 1 && lane < 40) dma16(L_PAR, lane * 16, mkrs(Ash, D * 4 * 2), hq * 640);
      if (w == 2 && lane < 10) dma16(L_PAR + 768, lane * 16, mkrs(AD, D * 2), hq * 160);
    }
    u64 ep, pc;
    u32 pfs, len;
    asm volatile("s_getpc_b64 %0\n.Lpf_pc%=:\n\ts_mov_b32 %1, .Lpf_end-.Lpf_pc%=" : "=s"(pc), "=s"(len));
    pc += umin_((u32)(bj * 8 + w) * 192, len - 0x88);
    asm volatile("s_load_dwordx2 %0, %2, 0x0\n\ts_load_dword %1, %3, 0x0\n\ts_load_dword %1, %3, 0x40\n\ts_load_dword %1, %3, 0x80"
                 : "=&s"(ep), "=&s"(pfs) : "s"(w == 7 ? (u64)(ws + WS_E) : pc), "s"(pc) : "memory");
    u32 wv = wsrc(w);
    asm volatile("s_barrier" : "+v"(wv)::"memory");
    if (w < 3) {
#pragma unroll
      for (int i = 0; i < 4; ++i) dma_w(0, i, w, wv);
#pragma unroll
      for (int i = 0; i < 4; ++i) dma_x(0, 4 * w + i);
#pragma unroll
      for (int i = 0; i < 4; ++i) dma_x(1, 4 * w + i);
#pragma unroll
      for (int i = 0; i < 4; ++i) dma_w(1, i, w, wv);
#pragma unroll
      for (int i = 0; i < 4; ++i) dma_x(2, 4 * w + i);
    } else {
      const int j = w - 3;
      u32 rb = L_WK + j * 5120 + (8 * gq + ((lane >> 2) & 3)) * 40 + 8 * (lane & 3);
      u32 xr[4], pa = L_PART + j * 2048 + lane * 16;
#pragma unroll
      for (int ks = 0; ks < 4; ++ks) xr[ks] = L_X + (j * 16 + 4 * rbk + (l15 >> 2)) * 1056 + (l15 & 3) * 256 + gq * 16 + ((ks ^ (l15 & 3)) * 64);
      const u32 rec = WS_REC + g * 7168, T = rbk >> 1, key = 16 * (rbk & 1) + l15 + 32 * gq;
      u32 sa0 = (hq & 1) ? rec + 3072 + rbk * 1024 + (gq >> 1) * 512 + ((gq & 1) ? 16 + l15 : swap23(l15)) * 16 : (gq < 2 ? rec + T * 1024 + key * 16 : OOB);
      u32 sa1 = gq < 2 ? rec + 2048 + T * 512 + key * 8 : OOB;
      wv0 = wsrc(0); wv1 = wsrc(1); wv2 = wsrc(2);
      asm volatile("s_waitcnt vmcnt(0) lgkmcnt(0)"
                   : "+v"(rb), "+v"(xr[0]), "+v"(xr[1]), "+v"(xr[2]), "+v"(xr[3]), "+v"(pa), "+v"(sa0), "+v"(sa1), "+v"(wv0), "+v"(wv1), "+v"(wv2)::"memory");
      STAMP(14);
      f32x4 a0 = {0.f, 0.f, 0.f, 0.f}, a1v = a0;
      bf16x8 w0[4], w1[4], xk[4];
#pragma unroll
      for (int ks = 0; ks < 4; ++ks) {
        xk[ks] = *lds<bf16x8>(xr[ks]);
        w0[ks] = cat8(trread(rb + ks * 1280), trread(rb + ks * 1280 + 160));
        w1[ks] = cat8(trread(rb + ks * 1280 + 32), trread(rb + ks * 1280 + 192));
      }
      SB();
      if (hq & 1) {
#pragma unroll
        for (int ks = 0; ks < 4; ++ks) {
          a0 = __builtin_amdgcn_mfma_f32_16x16x32_bf16(xk[ks], w0[ks], a0, 0, 0, 0);
          a1v = __builtin_amdgcn_mfma_f32_16x16x32_bf16(xk[ks], w1[ks], a1v, 0, 0, 0);
        }
      } else {
#pragma unroll
        for (int ks = 0; ks < 4; ++ks) {
          a0 = __builtin_amdgcn_mfma_f32_16x16x32_bf16(w0[ks], xk[ks], a0, 0, 0, 0);
          a1v = __builtin_amdgcn_mfma_f32_16x16x32_bf16(w1[ks], xk[ks], a1v, 0, 0, 0);
        }
      }
      if (__builtin_expect(w < 7, 1)) {
        *lds<f32x4>(pa) = a0;
        *lds<f32x4>(pa + 1024) = a1v;
        lds_arrive(CNT(C_PART));
      }
      asm volatile("" ::: "memory");
      if (__builtin_expect(w == 7, 1)) {
        __builtin_amdgcn_s_setprio(3);
        u32 pb = pa - 8192;
        asm volatile("" : "+v"(pb));
        lds_wait(CNT(C_PART), 4);
        f32x4 p0[4], p1[4];
#pragma unroll
        for (int i = 0; i < 4; ++i) {
          p0[i] = *lds<f32x4>(pb + i * 2048);
          p1[i] = *lds<f32x4>(pb + i * 2048 + 1024);
        }
        SB();
        a0 += (p0[0] + p0[1]) + (p0[2] + p0[3]);
        a1v += (p1[0] + p1[1]) + (p1[2] + p1[3]);
        u32x2 o0 = {pk2(a0[0], a0[1]), pk2(a0[2], a0[3])}, o1 = {pk2(a1v[0], a1v[1]), pk2(a1v[2], a1v[3])};
        if (hq & 1) {
          if (l15 == 4) o1 = (u32x2){0x3f803f80u, 0x3f803f80u};
          if (l15 > 4) o1 = (u32x2){0u, 0u};
          const auto x = __builtin_amdgcn_permlane16_swap(o0[0], o1[0], false, false);
          const auto y = __builtin_amdgcn_permlane16_swap(o0[1], o1[1], false, false);
          __builtin_amdgcn_raw_buffer_store_b128((u32x4){x[0], y[0], x[1], y[1]}, rsW, sa0, 0, SC1);
        } else {
          const float c = 0.22360679774997896f * 1.4426950408889634f;
          o0 = (u32x2){pk2(lo2f(o0[0]) * c, hi2f(o0[0]) * c), pk2(lo2f(o0[1]) * c, hi2f(o0[1]) * c)};
          o1 = (u32x2){pk2(lo2f(o1[0]) * c, hi2f(o1[0]) * c), pk2(lo2f(o1[1]) * c, hi2f(o1[1]) * c)};
          if (gq == 1) o1 = (u32x2){0x3f803f80u, 0x00003f80u};
          const auto x = __builtin_amdgcn_permlane32_swap(o0[0], o0[0], false, false);
          const auto y = __builtin_amdgcn_permlane32_swap(o0[1], o0[1], false, false);
          __builtin_amdgcn_raw_buffer_store_b128((u32x4){x[0], y[0], x[1], y[1]}, rsW, sa0, 0, SC1);
          __builtin_amdgcn_raw_buffer_store_b64(o1, rsW, sa1, 0, SC1);
        }
        STAMP(17);
      }
    }
    asm volatile("s_waitcnt lgkmcnt(0)" : "+s"(ep) : "s"(pfs) : "memory");
    tag = ep + 0x9E3779B97F4A7C15ull;
  }
  auto get_tag = [&]() {
    const u32x2 tv = *lds<u32x2>(L_TAG);
    tag = (u64)(u32)__builtin_amdgcn_readfirstlane(tv[0]) | ((u64)(u32)__builtin_amdgcn_readfirstlane(tv[1]) << 32);
  };
  const u32 oo = WS_O + g * GS + (((wq4 & 2) + (l31 >> 4)) * 20 + 10 * half) * 1024 + l15 * 16;
  u32 oa0 = oo + hq * 1024 + (2 * (wq4 & 1) + h) * 256, oa1 = h ? OOB : oo + (8 + (hq >> 2)) * 1024 + (hq & 3) * 256 + (wq4 & 1) * 8;
  auto put_o = [&](u32x4 o0, u32x2 o1) {
    __builtin_amdgcn_raw_buffer_store_b128(o0, rsW, oa0, 0, SC1);
    __builtin_amdgcn_raw_buffer_store_b64(o1, rsW, oa1, 0, SC1);
  };
  auto chan = [&](int c) { return 5 * (g >> 1) + (c == 0 ? 2 : 3 * (g & 1) + c - 1); };
  if (__builtin_expect(half == 1, 0)) {
    bf16x8 xy[5][4];
    u32x4 tm[3][2];
    u32 dd[3];
    float vf[3][8];
    const u32 xyb = L_X + (l15 >> 2) * 1056 + (l15 & 3) * 256 + gq * 16 + ((wq4 ^ (l15 & 3)) * 64);
    const u32 mv = (lane >> 2) * 1280 + ((lane & 3) ^ (2 * h)) * 16;
    auto mdma = [&](int i) {
      dma16(L_WM + wq4 * 4096 + (i & 3) * 1024, mv, rsQ, (mod5(rot + (i >> 1)) * 128 + 32 * wq4 + 16 * (i & 1)) * 1280 + hq * 160 + (hp ? 0 : 96));
    };
    u32 qa0 = qaddr(hp ? 0 : 3), qa1 = qaddr(hp ? 1 : 4), wm0 = L_WM + wq4 * 4096 + (8 * gq + ((lane >> 2) & 3)) * 64 + 8 * (lane & 3) + (gq & 1) * 32, wm1 = wm0 ^ 32;
    f32x4 ma[4][2];
    auto mstage = [&](int st) {
      const bf16x8 w0 = cat8(trread(wm0 + (st & 1) * 2048), trread(wm0 + (st & 1) * 2048 + 256));
      const bf16x8 w1 = cat8(trread(wm1 + (st & 1) * 2048), trread(wm1 + (st & 1) * 2048 + 256));
      SB();
      __builtin_amdgcn_s_setprio(MPRIO);
#pragma unroll
      for (int i = 0; i < 4; ++i) {
        ma[i][0] = __builtin_amdgcn_mfma_f32_16x16x32_bf16(w0, xy[st][i], st ? ma[i][0] : (f32x4){0.f, 0.f, 0.f, 0.f}, 0, 0, 0);
        ma[i][1] = __builtin_amdgcn_mfma_f32_16x16x32_bf16(w1, xy[st][i], st ? ma[i][1] : (f32x4){0.f, 0.f, 0.f, 0.f}, 0, 0, 0);
      }
      SB();
      __builtin_amdgcn_s_setprio(3);
    };
    {
      auto duty = [&](int b) {
        dma_w(b, wq4, 0, wv0);
        dma_w(b, wq4, 1, wv1);
        dma_w(b, wq4, 2, wv2);
        if (b < 4) {
#pragma unroll
          for (int i = 0; i < 3; ++i) dma_x(b + 1, 3 * wq4 + i);
        }
      };
      auto rdx = [&](int s) {
#pragma unroll
        for (int i = 0; i < 4; ++i) xy[s][i] = *lds<bf16x8>(xyb + ((wq4 + 1 + i) & 3) * 4224 + mod5(rot + s) * 16896);
      };
      auto zq = [&]() {
        *lds<u32x2>(L_QF + 4096 + wq4 * 512 + lane * 8) = (u32x2){0u, 0u};
        *lds<u32x2>(L_QF + 6144 + 4096 + wq4 * 512 + lane * 8) = (u32x2){0u, 0u};
      };
      STAMP(13);
      auto publish = [&]() {
        if (lane < NREP) put_flag(ws + WS_FKV + lane * 2048 + (g * 8 + hq) * 8, tag);
        STAMP(1);
      };
      if (w == 7) {
        if (lane == 0) *lds<u64>(L_TAG) = tag;
        if (PUBPOS == 0) { vm0(); publish(); }
      }
      duty(2);
      if (PUBPOS == 1 && w == 7) { vmw<6>(); publish(); }
      STAMP(15);
      bar();
      STAMP(16);
      if (PUBPOS == 2 && w == 7) { vmw<6>(); publish(); }
      get_tag();
      duty(3);
      rdx(0);
      rdx(1);
      bar();
      rdx(2);
#pragma unroll
      for (int i = 0; i < 3; ++i) {
        const u32 tap = (u32)*lds<u16>(L_PAR + (16 * (wq4 >> 1) + l15) * 8 + i * 256 + (3 - gq) * 2) << (16 * (l15 & 1));
        dd[i] = (u32)*lds<u16>(L_PAR + 768 + (16 * (wq4 >> 1) + l15) * 2 + i * 64) << 16;
#pragma unroll
        for (int e = 0; e < 8; ++e) tm[i][e >> 2][e & 3] = (l15 >> 1) == e ? tap : 0u;
        asm volatile("" : "+v"(tm[i][0]), "+v"(tm[i][1]), "+v"(dd[i]));
#pragma unroll
        for (int j = 0; j < 8; ++j) { vf[i][j] = 0.f; asm volatile("" : "+v"(vf[i][j])); }
      }
      asm volatile("" : "+v"(qa0), "+v"(qa1), "+v"(wm0), "+v"(wm1), "+v"(oa0), "+v"(oa1));
      vmw<6>();
      bar();
      rdx(3);
      duty(4);
      vmw<3>();
      bar();
      rdx(4);
      vm0();
      bar();
      STAMP(19);
      if (KLATE == 2) { if (h) zq(); bar(); }
      __builtin_amdgcn_s_sleep(KSLEEP);
#pragma unroll
      for (int i = 0; i < 2 * NE; ++i) mdma(i);
      if (KLATE == 1) { if (h) zq(); bar(); }
    if (lane < 60) {
      const rsrc_t rsAk = mkrs(Ak, S * D * 2);
      const int rk = 64 * g - 3 + 18 * wq4;
      dma16nt(L_KV + wq4 * 2880, rk < 0 ? (r6 < 3 ? OOB : vo6 - 3840) : vo6, rsAk, rk < 0 ? 0 : rk * 1280);
      dma16nt(L_KV + wq4 * 2880 + 960, vo6, rsAk, (rk + 6) * 1280);
      dma16nt(L_KV + wq4 * 2880 + 1920, (wq4 == 3 && r6 > 0) ? OOB : vo6, rsAk, (rk + 12) * 1280);
    }
      if (KLATE == 0) { if (h) zq(); bar(); }
      STAMP(4);
    }
    __builtin_amdgcn_s_setprio(3);
    __builtin_amdgcn_s_sleep(VSLEEP);
    tile64(L_KV + 11520, Av);
    if (NE) {
      vmw<5>();
#pragma unroll
      for (int st = 0; st < NE; ++st) mstage(st);
    }
    auto ymark = [&]() {
#pragma unroll
      for (int i = 0; i < 3; ++i)
        if (i < 2 || wq4 < 2)
          __builtin_amdgcn_raw_buffer_store_b128((u32x4){~0u, ~0u, ~0u, ~0u}, rsW,
              WS_YT + hq * HS + (g >> 2) * 40960 + (8 * wq4 + (lane >> 3)) * 512 + (g & 3) * 128 + (lane & 7) * 16 + i * 16384, 0, YMAUX);
    };
    STAMP(40);
    vm0();
    lds_arrive(CNT(C_KT));
    const rsrc_t rsH = mkrs(Assm, D * S * 2);
    const u32 hrow = wq4 < 3 ? (hq * 80 + chan(wq4)) * 4096 : OOB;
    u32x4 ht[4];
#pragma unroll
    for (int i = 0; i < 4; ++i) ht[i] = __builtin_amdgcn_raw_buffer_load_b128(rsH, lane * 16 + i * 1024, hrow, HAUX);
    if (YMPOS == 0) ymark();
    lds_wait<1>(CNT(C_KT), 4);
    STAMP(41);
    {
      const u32 run = 4 * (wq4 & 1) + gq;
      const u32 ka = L_KV + 8 * run * 160 + (wq4 >> 1) * 32 + 2 * l15;
      const u32 sa = L_A1 + (16 * (wq4 >> 1) + l15) * 128 + ((7 - run) ^ (l15 & 7)) * 16;
      const u32 cr = 8 * (wq4 & 1) + (lane >> 3), sl = lane & 7;
      const u32 ra = L_A1 + (16 * (wq4 >> 1) + cr) * 128 + (sl ^ (cr & 7)) * 16;
      const u32 so = WS_KVT + hq * HS + (7 - (g >> 2)) * 40960 + (16 * (wq4 >> 1) + cr) * 512 + (3 - (g & 3)) * 128 + sl * 16;
      u32x4 md[3];
      const u32 kb = L_KV + (32 * (wq4 & 1) + 8 * (l15 >> 2) + (l15 & 3) + gq) * 160 + (wq4 >> 1) * 32;
      f32x4 ks[3][2];
      auto prod = [&](auto I) {
        constexpr int i = decltype(I)::value;
        bf16x8 kr[2][2];
#pragma unroll
        for (int n = 0; n < 4; ++n) kr[n >> 1][n & 1] = *lds<bf16x8>(kb + i * 64 + (n >> 1) * 640 + (n & 1) * 16);
        SB();
#pragma unroll
        for (int e = 0; e < 2; ++e)
#pragma unroll
          for (int T = 0; T < 2; ++T)
            ks[i][T] = __builtin_amdgcn_mfma_f32_16x16x32_bf16(kr[T][e], __builtin_bit_cast(bf16x8, tm[i][e]), e ? ks[i][T] : (f32x4){0.f, 0.f, 0.f, 0.f}, 0, 0, 0);
        static_for<8>([&](auto J) { d16hi<11520 + i * 64 + decltype(J)::value * 160>(vf[i][decltype(J)::value], ka); });
      };
      auto calc = [&](int i) {
        float(&v)[8] = vf[i];
        asm volatile("s_waitcnt lgkmcnt(0)\n\ts_nop 7"
                     : "+v"(v[0]), "+v"(v[1]), "+v"(v[2]), "+v"(v[3]), "+v"(v[4]), "+v"(v[5]), "+v"(v[6]), "+v"(v[7]), "+v"(ks[i][0]), "+v"(ks[i][1])::"memory");
        float r[8];
#pragma unroll
        for (int j = 0; j < 8; ++j) r[j] = mul1(ks[i][j >> 2][j & 3], v[j]);
        *lds<u32x4>(sa + i * 4096) = (u32x4){pk2(r[7], r[6]), pk2(r[5], r[4]), pk2(r[3], r[2]), pk2(r[1], r[0])};
#pragma unroll
        for (int j = 0; j < 8; ++j) r[j] = mul1(r[j], asf(dd[i]));
        md[i] = (u32x4){pk2(r[0], r[1]), pk2(r[2], r[3]), pk2(r[4], r[5]), pk2(r[6], r[7])};
        SB();
      };
      prod(IC<0>{});
      prod(IC<1>{});
      if (wq4 < 2) prod(IC<2>{});
      calc(0);
      calc(1);
      if (wq4 < 2) calc(2);
      lgkm0();
      STAMP(42);
      if (YMPOS == 1) ymark();
      lds_arrive(CNT(C_K0) + (wq4 >> 1) * 4);
      lds_wait(CNT(C_K0) + (wq4 >> 1) * 4, 2);
#pragma unroll
      for (int i = 0; i < 3; ++i)
        if (i < 2 || wq4 < 2) __builtin_amdgcn_raw_buffer_store_b128(*lds<u32x4>(ra + i * 4096), rsW, so + i * 16384, 0, SC1);
      lds_arrive(CNT(C_K0) + (wq4 >> 1) * 4);
      lds_wait(CNT(C_K0) + (wq4 >> 1) * 4, 4);
#pragma unroll
      for (int i = 0; i < 3; ++i)
        if (i < 2 || wq4 < 2) *lds<u32x4>(sa + i * 4096) = md[i];
    }
    const u32 img = L_CV + wq4 * CV_CH;
    auto put_h = [&](u32 a, const u32x4 (&ht)[4]) {
#pragma unroll
      for (int i = 0; i < 4; ++i) {
        *lds<u32x4>(a + 32 + i * 1024) = ht[i];
        asm volatile("ds_write2_b32 %0, %1, %2 offset0:7 offset1:8\n\tds_write2_b32 %0, %3, %4 offset0:9 offset1:10"
                     ::"v"(a + 8320 + i * 1024), "v"(ht[i][0]), "v"(ht[i][1]), "v"(ht[i][2]), "v"(ht[i][3]) : "memory");
      }
    };
    asm volatile("" : "+v"(ht[3])::"memory");
#pragma unroll
    for (int i = 0; i < 2; ++i) mdma(2 * NE + i);
    if (wq4 < 3) {
      if (lane < 40) *lds<u32x4>(img + (lane < 8 ? (lane >> 1) * 4160 + (lane & 1) * 16 : 16640 + 4096 + (lane - 8) * 16)) = (u32x4){0u, 0u, 0u, 0u};
      lgkm0();
      if (lane == 0) {
        *lds<u16>(img + 4160 + 30) = (u16)ht[0][0];
        *lds<u16>(img + 3 * 4160 + 26) = (u16)ht[0][0];
      }
      put_h(img + lane * 16, ht);
      u32 hn[4];
#pragma unroll
      for (int i = 0; i < 4; ++i) hn[i] = *lds<u32>(img + 48 + i * 1024 + lane * 16);
#pragma unroll
      for (int i = 0; i < 4; ++i)
        ht[i] = (u32x4){__builtin_amdgcn_alignbit(ht[i][1], ht[i][0], 16), __builtin_amdgcn_alignbit(ht[i][2], ht[i][1], 16),
                        __builtin_amdgcn_alignbit(ht[i][3], ht[i][2], 16), __builtin_amdgcn_alignbit(hn[i], ht[i][3], 16)};
    }
    vmw<2>();
    if (lane == 0) put_flag(ws + WS_FA1 + ((hq * 4 + wq4) * 32 + g) * FSA, tag);
    STAMP(2);
    if (wq4 < 3) put_h(img + 4160 + lane * 16, ht);
    u32x4(&t)[4] = ht;
#pragma unroll
    for (int st = 1; st < 4; ++st) {
      vm0();
      mdma(2 * st + 2);
      mdma(2 * st + 3);
      mstage(st);
    }
    if (wq4 < 3) wait_flags<ASLEEP>(ws + WS_FA1 + ((hq * 4 + ((chan(wq4) >> 3) & 2) + h) * 32 + l31) * FSA, true, tag);
    STAMP(43);
    asm volatile("s_waitcnt vmcnt(0) lgkmcnt(0)" ::: "memory");
#pragma unroll
    for (int i = 0; i < 4; ++i)
      t[i] = __builtin_amdgcn_raw_buffer_load_b128(rsW, h * 40960 + l31 * 16, wq4 < 3 ? WS_KVT + hq * HS + (2 * i) * 40960 + chan(wq4) * 512 : OOB, SC1);
    SB();
    mstage(4);
#pragma unroll
    for (int i = 0; i < 3; ++i) {
      const u32 a = L_WM + 16384 + (((wq4 + 1 + i) & 3) * 3 + i) * 2048 + lane * 16;
      *lds<f32x4>(a) = ma[i][0];
      *lds<f32x4>(a + 1024) = ma[i][1];
    }
    SB();
    if (wq4 < 3) {
#pragma unroll
      for (int i = 0; i < 4; ++i) *lds<u32x4>(img + 16640 + lane * 16 + i * 1024) = t[i];
    }
    STAMP(44);
    lds_arrive(CNT(C_WM));
    put_o((u32x4){~0u, ~0u, ~0u, ~0u}, (u32x2){~0u, ~0u});
    tile64(L_QT, Aq);
    lds_wait<1>(CNT(C_WM), 4);
    {
      f32x4 p[6];
#pragma unroll
      for (int i = 0; i < 6; ++i) p[i] = *lds<f32x4>(L_WM + 16384 + wq4 * 6144 + i * 1024 + lane * 16);
      SB();
      const f32x4 a0 = add4(add4(ma[3][0], p[0]), add4(p[2], p[4])), a1 = add4(add4(ma[3][1], p[1]), add4(p[3], p[5]));
      *lds<u32x2>(qa0) = (u32x2){pk2(a0[0], a0[1]), pk2(a0[2], a0[3])};
      *lds<u32x2>(qa1) = (u32x2){pk2(a1[0], a1[1]), pk2(a1[2], a1[3])};
      lds_arrive(CNT(C_CS));
    }
    STAMP(5);
    if (g & 1) conv_shared<0x3B, 6>(L_CV, wq4, lane);
    else conv_shared<0xC4, 8>(L_CV, wq4, lane);
    LOAD_H1(A, rec0);
    LOAD_H2(A, rec0);
#pragma unroll
    for (int i = 0; i < 16; ++i) {
      const f32x4 z = *(volatile LAS f32x4*)(size_t)(L_CV + CV_CH + 16640 + 4096);
#pragma unroll
      for (int j = 0; j < 4; ++j) O[i >> 2][4 * (i & 3) + j] = z[j];
    }
    lds_wait<1>(CNT(C_CS), 4);
    read_q();
    STAMP(6);
    STAMP(7); CSTAMP(30);
    vmw<8>();
    lds_arrive(CNT(C_CH));
    LOAD_H1(B, rec0 + 7168);
    LOAD_H2(B, rec0 + 7168);
    __builtin_amdgcn_s_setprio(1);
  } else {
    f32x4 acc[3];
    u32 qa[3];
#pragma unroll
    for (int i = 0; i < 3; ++i) {
      acc[i] = (f32x4){0.f, 0.f, 0.f, 0.f};
      qa[i] = qaddr(2 * hp + i);
    }
    const u32 wl = L_RING + (16 * (gq >> 1) + 4 * (gq & 1) + ((lane >> 2) & 3)) * 96 + 8 * (lane & 3);
    bf16x8 xf[3][4], wf[2][4][3];
    u64 fl = 0;
    zero_o();
    u32 kp2[2], jo2[2];
#pragma unroll
    for (int i = 0; i < 2; ++i) {
      const u32 c = (i ? 256 : 64 * wq4) + lane, pos = c / 10;
      kp2[i] = swap23(pos);
      jo2[i] = (c - pos * 10) * 16;
    }
    vm0_ = (kp2[0] + (kp2[0] >> 4) * 4) * 1280 + jo2[0]; vm1_ = (kp2[1] + 4) * 1280 + jo2[1];
    auto trow = [&](u32 kp) { return (kp >> 3) * 80 + ((kp >> 2) & 1) * 20 + 16 + (kp & 3); };
    vt0 = (trow(kp2[0]) + (kp2[0] >> 4) * 40) * 1280 + jo2[0]; vt2 = (trow(kp2[0]) + 40 - (kp2[0] >> 4) * 40) * 1280 + jo2[0];
    vt1 = trow(kp2[1]) * 1280 + jo2[1];
    asm volatile("" : "+v"(vm0_), "+v"(vm1_), "+v"(vt0), "+v"(vt1), "+v"(vt2), "+v"(oa0), "+v"(oa1));
#pragma unroll
    for (int s = 0; s < 5; ++s) {
      if (s == 0) vmw<8>();
      if (s == 1) vm0();
      if (s == 0) STAMP(15);
      bar();
      if (s == 0) STAMP(16);
      if (s == 1) STAMP(18);
      if (s == 4) STAMP(19);
      if (s == 1) get_tag();
      if (s == 2) fl = __hip_atomic_load((GAS u64*)kvflag, __ATOMIC_RELAXED, __HIP_MEMORY_SCOPE_AGENT);
      const u32 xo = mod5(rot + s + 1) * 16896;
      auto rdw = [&](int ks) {
#pragma unroll
        for (int i = 0; i < 3; ++i) {
          const u32 wb = wl + s * 12288 + ks * 3072 + i * 32;
          wf[s & 1][ks][i] = cat8(trread(wb), trread(wb + 768));
        }
        if (s == 0) xf[0][ks] = *lds<bf16x8>(xa[ks] + rot * 16896);
        if (s < 4) xf[(s + 1) % 3][ks] = *lds<bf16x8>(xa[ks] + xo);
      };
      auto mma = [&](int st, int ks) {
#pragma unroll
        for (int i = 0; i < 3; ++i) acc[i] = __builtin_amdgcn_mfma_f32_16x16x32_bf16(wf[st & 1][ks][i], xf[st % 3][ks], acc[i], 0, 0, 0);
      };
#pragma unroll
      for (int ks = 0; ks < 4; ++ks) {
        rdw(ks);
        SB();
        if (s > 0 && s < 4) mma(s - 1, ks);
        SB();
      }
      if (s == 3) {
        if (!__all(fl == tag)) wait_flags<4>(kvflag, true, tag);
        asm volatile("" ::: "memory");
        STAMP(7); CSTAMP(30);
        LOAD_H1(A, rec0);
        SB();
      }
      if (s == 4) { LOAD_H1(B, rec0 + 7168); SB(); }
      if (s >= 3) {
#pragma unroll
        for (int ks = 0; ks < 4; ++ks) mma(s, ks);
        SB();
      }
      if (s == 3) LOAD_H2(A, rec0);
    }
    STAMP(3);
#pragma unroll
    for (int i = 0; i < 3; ++i) *lds<u32x2>(qa[i]) = (u32x2){pk2(acc[i][0], acc[i][1]), pk2(acc[i][2], acc[i][3])};
    LOAD_H2(B, rec0 + 7168);
    bar();
    read_q();
    __builtin_amdgcn_s_setprio(2);
    STAMP(4);
  }
  u32x4 yv[4];
  auto yload = [&]() {
#pragma unroll
    for (int i = 0; i < 4; ++i)
      yv[i] = __builtin_amdgcn_raw_buffer_load_b128(rsW, (lane >> 3) * 512 + (lane & 7) * 16,
                                                    wq4 * 4 + i < 10 ? WS_YT + hq * HS + ((g >> 2)) * 40960 + (g & 3) * 128 + (wq4 * 4 + i) * 4096 : OOB, SC1);
    SB();
  };
  {
    const int idx = (int)0xDDDDDDDDu;
    const u32 cntA = half ? CNT(C_AT1) : CNT(C_AT0);
    WAIT_H1(A, 12);
    f32x16 Sx[4];
    Sx[0] = qk16(A.k1a, q1[0]); SB();
    Sx[0] = qk8(A.k2a, q2[0], Sx[0]); SB();
    Sx[1] = qk16(A.k1a, q1[1]); SB();
    Sx[1] = qk8(A.k2a, q2[1], Sx[1]); SB();
    STAMP(28); LSTAMP(47);
    __builtin_amdgcn_s_waitcnt(0xC07F);
    SB();
    REC_STEPS(A, B, 0); REC_STEPS(B, A, 1); REC_STEPS(A, B, 2); REC_STEPS(B, A, 3);
    REC_STEPS(A, B, 4); REC_STEPS(B, A, 5); REC_STEPS(A, B, 6);
    const u32 ored = half ? L_OY : L_OX;
    u32 ga;
    auto give = [&](int i, int j) {
      if (j == 0) { ga = ored + (((wq4 + 1 + i) & 3) * 3 + i) * 3072 + lane * 16; asm volatile("" : "+v"(ga)); }
      *lds<f32x4>(ga + j * 1024) = (f32x4){O[i][4 * j], O[i][4 * j + 1], O[i][4 * j + 2], O[i][4 * j + 3]};
    };
    u32 cdv;
    const u32 cfree = half ? CNT(C_CD) : CNT(C_CS);
    STEPX(0, 0, 3, 1, A.v3, B.v0, B.k1a, B.k2a, 1, , cdv = *(volatile LAS u32*)(size_t)cfree);
    STEPX(1, 0, 0, 1, B.v1, B.v2, B.k1b, B.k2b, 1, ,
          if (__builtin_expect(__builtin_amdgcn_readfirstlane(cdv) < 4, 0)) lds_wait<1>(cfree, 4));
    STEPX(2, 1, 0, 2, B.v3, B.v0, B.k1a, B.k2a, 1, , give(0, 0));
    STEPX(3, 1, 1, 2, B.v1, B.v2, B.k1b, B.k2b, 1, give(0, 1), give(0, 2));
    STEPX(0, 2, 1, 3, B.v3, B.v0, B.k1a, B.k2a, 1, , give(1, 0));
    STEPX(1, 2, 2, 3, B.v1, B.v2, B.k1b, B.k2b, 1, give(1, 1), give(1, 2));
    STEPX(2, 3, 2, 0, B.v3, B.v0, B.k1a, B.k2a, 0, , give(2, 0); give(2, 1); give(2, 2));
    EXP4(Sx[3], 0); SB();
    O[3] = pv_hi(B.v1, Sx[2], O[3], idx); SB();
    EXP4(Sx[3], 4); SB();
    EXP4(Sx[3], 8); SB();
    O[3] = pv_lo(B.v2, Sx[3], O[3], idx); SB();
    lds_arrive(cntA);
    EXP4(Sx[3], 12); SB();
    O[3] = pv_hi(B.v3, Sx[3], O[3], idx); SB();
    STAMP(8); CSTAMP(31);
    lds_wait(cntA, 4);
    STAMP(21);
    f32x4 pr[9], t[3];
#pragma unroll
    for (int i = 0; i < 9; ++i) pr[i] = *lds<f32x4>(ored + wq4 * 9216 + lane * 16 + (8 - i) * 1024);
    SB();
    if (half == 1) yload();
#pragma unroll
    for (int i = 0; i < 3; ++i) t[i] = add4(add4((f32x4){O[3][4 * i], O[3][4 * i + 1], O[3][4 * i + 2], O[3][4 * i + 3]}, pr[2 - i]), add4(pr[5 - i], pr[8 - i]));
    float lsum = upper_half(t[2][0]);
    if (__builtin_expect(!__all(lsum >= 5.421010862427522e-20f && lsum <= 1.8446744073709552e19f), 0)) {
      float m = -INFINITY;
#pragma unroll 1
      for (int r = 0; r < 64; ++r) {
        const u32 so = WS_REC + (r >> 1) * 7168;
        const f32x16 sa = qk8(LD64(2048, so + (r & 1) * 512), q2[3], qk16(LD128(0, so + (r & 1) * 1024), q1[3]));
#pragma unroll
        for (int i = 0; i < 16; ++i) m = fmaxf(m, sa[i]);
      }
      const auto mm = __builtin_amdgcn_permlane32_swap(__builtin_bit_cast(u32, m), __builtin_bit_cast(u32, m), false, false);
      const float M = -fmaxf(asf(mm[0]), asf(mm[1]));
      const u32 m0 = __builtin_bit_cast(u32, M) & 0xffff0000u;
      const float r1 = M - __builtin_bit_cast(float, m0);
      const u32 m1 = __builtin_bit_cast(u32, r1) & 0xffff0000u;
      const float r2 = r1 - __builtin_bit_cast(float, m1);
      const u32x2 qs = h ? (u32x2){(m0 >> 16) | m1, __builtin_bit_cast(u32, r2) >> 16} : q2[3];
      f32x16 o;
#pragma unroll
      for (int i = 0; i < 16; ++i) o[i] = 0.f;
#pragma unroll 1
      for (int r = 0; r < 64; ++r) {
        const u32 so = WS_REC + (r >> 1) * 7168;
        f32x16 p = qk8(LD64(2048, so + (r & 1) * 512), qs, qk16(LD128(0, so + (r & 1) * 1024), q1[3]));
        const u32x4 va = LDV(0, so + 3072 + (r & 1) * 2048), vb = LDV(1024, so + 3072 + (r & 1) * 2048);
        EXP4(p, 0); EXP4(p, 4); EXP4(p, 8); EXP4(p, 12);
        o = pv_hi(vb, p, pv_lo(va, p, o, idx), idx);
      }
      asm volatile("s_nop 15" : "+v"(o));
#pragma unroll
      for (int i = 0; i < 3; ++i) t[i] = (f32x4){o[4 * i], o[4 * i + 1], o[4 * i + 2], o[4 * i + 3]};
      lsum = upper_half(o[8]);
    }
    float inv = __builtin_amdgcn_rcpf(lsum);
    asm volatile("s_nop 0" : "+v"(inv));
    auto fin = [&](float a, float b) { return pk2s(mul1(a, inv), mul1(b, inv)); };
    put_o((u32x4){fin(t[0][0], t[0][1]), fin(t[0][2], t[0][3]), fin(t[1][0], t[1][1]), fin(t[1][2], t[1][3])},
          (u32x2){fin(t[2][0], t[2][1]), fin(t[2][2], t[2][3])});
    STAMP(22);
  }
  char* const oflag = ws + WS_FO + ((g * 8 + hq) * 4 + wq4) * FSO;
  const rsrc_t rsOut = mkrs(out, S * 2 * D * 2);
  if (half == 1) {
    __builtin_amdgcn_s_sleep(PSLEEP);
    lds_arrive(CNT(C_PUB));
    STAMP(9);
    if (wq4 < 3) {
#pragma unroll 1
      for (u32 spins = 0; spins < SPIN_CAP; ++spins) {
        u32 mx = 0;
#pragma unroll
        for (int i = 0; i < 4; ++i) mx = umax_(umax_(umax_(umax_(mx, yv[i][0]), yv[i][1]), yv[i][2]), yv[i][3]);
        if (__all(mx != ~0u)) break;
        lds_wait<1>(CNT(C_A3), 8);
        yload();
      }
      lds_wait<1>(CNT(C_GO), 3);
      STAMP(23);
      const u32 scr = L_GO + wq4 * 8192 + 4096;
      u32x4 qq[4], md[4];
#pragma unroll
      for (int i = 0; i < 4; ++i) {
        qq[i] = *lds<u32x4>(scr - 4096 + i * 1024 + lane * 16);
        md[i] = *lds<u32x4>(scr + (wq4 < 2 ? i : (i & 1)) * 1024 + lane * 16);
      }
      lgkm0();
#pragma unroll
      for (int i = 0; i < 4; ++i) {
        u32x4 y2;
#pragma unroll
        for (int j = 0; j < 4; ++j) y2[j] = pk2(add1(lo2f(yv[i][j]), lo2f(md[i][j])), add1(hi2f(yv[i][j]), hi2f(md[i][j])));
        const u32 chl = 8 * i + (lane >> 3);
        if (wq4 < 2 || i < 2) *lds<u32x4>(scr + chl * 128 + ((lane & 7) ^ (chl >> 2)) * 16) = y2;
      }
      lgkm0();
      const u32 run = lane >> 3;
      u32x4 z[4];
      u32x2 res[8];
#pragma unroll
      for (int c = 0; c < 4; ++c) z[c] = *lds<u32x4>(scr + (((lane & 7) * 4 + c) & (wq4 < 2 ? 31 : 15)) * 128 + (run ^ (lane & 7)) * 16);
#pragma unroll
      for (int i = 0; i < 8; ++i) {
        float r[4];
#pragma unroll
        for (int c = 0; c < 4; ++c) {
          const u32 qv = qq[i >> 1][2 * (i & 1) + (c >> 1)];
          r[c] = mul1((i & 1) ? hi2f(z[c][i >> 1]) : lo2f(z[c][i >> 1]), (c & 1) ? hi2f(qv) : lo2f(qv));
        }
        res[i] = (u32x2){pk2(r[0], r[1]), pk2(r[2], r[3])};
      }
#pragma unroll
      for (int i = 0; i < 8; ++i) *lds<u32x2>(scr - 4096 + (8 * run + i) * 64 + ((lane & 7) ^ (run & 6)) * 8) = res[i];
      lgkm0();
      u32x4 fo[4];
#pragma unroll
      for (int i = 0; i < 4; ++i) fo[i] = *lds<u32x4>(scr - 4096 + i * 1024 + (lane >> 2) * 64 + ((lane & 3) ^ i) * 16);
      const u32 ob = wq4 * 4 + (lane & 3) < 10 ? (64 * g + (lane >> 2)) * 2560 + hq * 160 + wq4 * 64 + (lane & 3) * 16 : OOB;
      lds_wait<1>(CNT(HOLDC), HOLDN);
      asm volatile(".Lpf_end:");
#pragma unroll
      for (int i = 0; i < 4; ++i) __builtin_amdgcn_raw_buffer_store_b128(fo[i], rsOut, ob + i * 40960, 0, A3AUX);
    }
    __builtin_amdgcn_endpgm();
  }
  {
    if (blockIdx.x == 0 && threadIdx.x == 0) put_flag(ws + WS_E, tag);
    lds_wait<2>(CNT(C_CH), 4);
    u32x4 kvv[4];
    u32x2 qr[8];
#pragma unroll
    for (int i = 0; i < 8; ++i) qr[i] = *lds<u32x2>(L_QT + (8 * (lane >> 3) + i) * 160 + (w * 8 + (lane & 7)) * 8);
#pragma unroll
    for (int i = 0; i < 4; ++i) {
      const u32 chl = 8 * i + (lane >> 3);
      kvv[i] = *lds<u32x4>(L_A1 + w * 4096 + chl * 128 + ((7 - (lane & 7)) ^ (chl & 7)) * 16);
    }
    SB();
    const rsrc_t rsO = mkrs(Bwo, D * D * 2);
    auto oload = [&](u32x4 (&xf)[10], int kh) {
#pragma unroll
      for (int ks = 0; ks < 10; ++ks)
        xf[ks] = __builtin_amdgcn_raw_buffer_load_b128(rsW, lane * 16 + (ks & 3) * 1024, WS_O + g * GS + (w * 20 + 10 * kh + (ks & ~3)) * 1024, SC1);
      SB();
    };
    u32x4 xe[10];
    {
      const u32 img = L_CV + (1 + (wq4 >> 1)) * CV_CH;
      const u32 xb = conv_xb(img, lane) + (wq4 & 1) * 256, hb = conv_hb(img, lane) + (wq4 & 1) * 256;
      f32x4 acc[8];
#pragma unroll
      for (int i = 0; i < 8; ++i) acc[i] = (f32x4){0.f, 0.f, 0.f, 0.f};
      auto wo_dma = [&](int n) {
        const int q = (n / 5 + g) & 3, e = q >> 1, c = 4 * (q & 1) + (n % 5 < 4 ? n % 5 : wq4);
        if (n < 20) dma16(L_W + (10 * e + c) * 5120 + (n % 5 < 4 ? wq4 : 4) * 1024, n % 5 < 4 ? vm0_ : vm1_, rsO, (80 * c + 40 * (((c >> 1) & 1) ^ e)) * 1280 + hq * 160);
        else if (n < 24) dma16(L_W + (10 * ((n >> 1) & 1) + 8 + (n & 1)) * 5120 + wq4 * 1024, (n & 2) ? vt2 : vt0, rsO, (n & 1) * 320 * 1280 + hq * 160);
        else dma16(L_W + (10 * (wq4 >> 1) + 8 + (wq4 & 1)) * 5120 + 4096, vt1, rsO, ((wq4 & 1) * 320 + 40 * (1 - (wq4 >> 1))) * 1280 + hq * 160);
      };
      auto residue = [&](auto BB, const CFrag<8>& f, CFrag<8>& n) {
        constexpr int B = decltype(BB)::value;
        static_for<36>([&](auto NN) {
          constexpr int N = decltype(NN)::value, a = tri_a(N), c = 7 - a - (N - tri_0(a));
          acc[a + c] = __builtin_amdgcn_mfma_f32_16x16x32_bf16(__builtin_bit_cast(bf16x8, f.a[a]), __builtin_bit_cast(bf16x8, f.b[c]), acc[a + c], 0, 0, 0);
          if constexpr (B < 3 && N < 8) n.b[7 - N] = *lds<u32x4>(xb + 64 * (B + 1) + 512 * N);
          if constexpr (B < 3 && N >= 8 && N < 24) {
            const u32x2 p = *lds<u32x2>(hb + 64 * (B + 1) + 512 * ((N - 8) >> 1) + 8 * (N & 1));
            n.a[(N - 8) >> 1][2 * (N & 1)] = p[0];
            n.a[(N - 8) >> 1][2 * (N & 1) + 1] = p[1];
          }
          if constexpr (B > 0 && N % 4 == 0 && (B - 1) * 9 + N / 4 < 25) wo_dma((B - 1) * 9 + N / 4);
          SB();
        });
      };
      CFrag<8> f0, f1;
      conv_load(f0, hb, xb);
      SB();
    if (wq4 < 3) {
      const u32 n = 2 * wq4 + h;
      const u32 tile = (((g & 1) ? 0x54310u : 0x762u) >> (4 * n)) & 7;
      f32x4 p[8];
#pragma unroll
      for (int i = 0; i < 8; ++i) p[i] = *lds<f32x4>(L_CRED + (i >> 1) * 5120 + (i & 1) * 16 + wq4 * 2048 + lane * 32);
      SB();
      const f32x4 a = add4(add4(p[0], p[2]), add4(p[4], p[6])), b = add4(add4(p[1], p[3]), add4(p[5], p[7]));
      __builtin_amdgcn_raw_buffer_store_b128((u32x4){pk2s(a[0], a[1]), pk2s(a[2], a[3]), pk2s(b[0], b[1]), pk2s(b[2], b[3])}, rsW,
                                             n < ((g & 1) ? 5u : 3u) ? WS_YT + hq * HS + (tile) * 40960 + chan(0) * 512 + l31 * 16 : OOB, 0, SC1);
    }
      lds_arrive(CNT(C_OR));
      STAMP(5);
      __builtin_amdgcn_s_setprio(CP0);
      residue(IC<0>{}, f0, f1);
      __builtin_amdgcn_s_setprio(2);
      vm0();
      if (lane == 0) put_flag(oflag, tag);
      lds_wait<1>(CNT(C_OR), 4);
      const char* eflag = ws + WS_FO + ((g * 8 + (l15 >> 1)) * 4 + (w & 2) + (l15 & 1)) * FSO;
      asm volatile("" : "+v"(eflag));
      __builtin_amdgcn_s_setprio(CP1);
      residue(IC<1>{}, f1, f0);
      __builtin_amdgcn_s_setprio(CP2);
      residue(IC<2>{}, f0, f1);
      __builtin_amdgcn_s_setprio(CP3);
      residue(IC<3>{}, f1, f0);
      __builtin_amdgcn_s_setprio(2);
      const u64 ef = __hip_atomic_load((GAS u64*)eflag, __ATOMIC_RELAXED, __HIP_MEMORY_SCOPE_AGENT);
      SB();
      const u32 xch = L_CV + 4096;
      const u32 stg = img + (wq4 & 1) * 2048;
      auto swap_tiles = [&](auto KK) {
        constexpr int K = decltype(KK)::value;
#pragma unroll
        for (int i = 0; i < 4; ++i) *lds<f32x4>(xch + wq4 * 4096 + i * 1024 + lane * 16) = acc[4 - K + i];
        lds_arrive(CNT(C_P0) + (wq4 >> 1) * 4);
        lds_wait<1>(CNT(C_P0) + (wq4 >> 1) * 4, 2);
#pragma unroll
        for (int i = 0; i < 4; ++i) {
          const f32x4 v = add4(acc[K + i], *lds<f32x4>(xch + (wq4 ^ 1) * 4096 + i * 1024 + lane * 16));
          *lds<u32x2>(stg + 512 * i + 32 * l15 + 8 * gq) = (u32x2){pk2s(v[0], v[1]), pk2s(v[2], v[3])};
        }
      };
      if (wq4 & 1) swap_tiles(IC<4>{}); else swap_tiles(IC<0>{});
      lgkm0();
      const u32 yo = WS_YT + hq * HS + ((wq4 & 1) * 4 + h) * 40960 + chan(1 + (wq4 >> 1)) * 512 + l31 * 16;
      __builtin_amdgcn_raw_buffer_store_b128(*lds<u32x4>(stg + lane * 16), rsW, yo, 0, SC1);
      __builtin_amdgcn_raw_buffer_store_b128(*lds<u32x4>(stg + 1024 + lane * 16), rsW, yo + 81920, 0, SC1);
      vmw<2>();
      lds_arrive(CNT(C_CD));
      if (!__all(ef == tag)) wait_flags<2>(eflag, true, tag);
      asm volatile("" ::: "memory");
      oload(xe, 0);
    }
    STAMP(6);
    lds_wait<1>(CNT(C_CD), 4);
    if (w < 3) {
#pragma unroll
      for (int i = 0; i < 4; ++i) {
        *lds<u32x4>(L_GO + w * 8192 + i * 1024 + lane * 16) = (u32x4){qr[2 * i][0], qr[2 * i][1], qr[2 * i + 1][0], qr[2 * i + 1][1]};
        if (w < 2 || i < 2) *lds<u32x4>(L_GO + w * 8192 + 4096 + i * 1024 + lane * 16) = kvv[i];
      }
      lds_arrive(CNT(C_GO));
    }
    f32x4 acc[5];
#pragma unroll
    for (int i = 0; i < 5; ++i) acc[i] = (f32x4){0.f, 0.f, 0.f, 0.f};
    const u32 wl = L_W + (16 * (gq >> 1) + 4 * (gq & 1) + ((lane >> 2) & 3)) * 160 + 8 * (lane & 3);
    auto wfrag = [&](u32 wb, int ks, int nt) { return cat8(trread(wb + ks * 5120 + nt * 32), trread(wb + ks * 5120 + 1280 + nt * 32)); };
    {
      lds_wait<2>(CNT(C_AT1), EGN);
      STAMP(25);
      __builtin_amdgcn_s_setprio(EPRIO);
      const u32 wb = wl;
      bf16x8 wf[2][5];
#pragma unroll
      for (int nt = 0; nt < 5; ++nt) wf[0][nt] = wfrag(wb, 0, nt);
      SB();
#pragma unroll
      for (int ks = 0; ks < 10; ++ks) {
#pragma unroll
        for (int nt = 0; nt < 5; ++nt) {
          if (ks < 9) wf[(ks + 1) & 1][nt] = wfrag(wb, ks + 1, nt);
          acc[nt] = __builtin_amdgcn_mfma_f32_16x16x32_bf16(wf[ks & 1][nt], __builtin_bit_cast(bf16x8, xe[ks]), acc[nt], 0, 0, 0);
        }
        SB();
      }
      STAMP(26);
    }
    SB();
    {
      const int kh = 1;
      u32 wb = wl + kh * 51200;
      asm volatile("" : "+v"(wb));
      constexpr int NPRE = 4;
      bf16x8 wf[NPRE][5];
#pragma unroll
      for (int ks = 0; ks < NPRE; ++ks)
#pragma unroll
        for (int nt = 0; nt < 5; ++nt) wf[ks][nt] = wfrag(wb, ks, nt);
      SB();
      STAMP(10);
      lds_arrive(CNT(C_A3));
      STAMP(45);
      __builtin_amdgcn_s_setprio(2);
      lds_wait<1>(CNT(C_A3), 4);
      lds_wait<1>(CNT(C_PUB), 4);
      f32x4 res[5];
#pragma unroll 1
      for (u32 spins = 0; spins < SPIN_CAP; ++spins) {
        u32x4 xf[10];
        asm volatile("" ::: "memory");
        oload(xf, kh);
        lds_arrive(CNT(C_SW));
        bf16x8 wl2[2][5];
#pragma unroll
        for (int ks = 0; ks < 2; ++ks)
#pragma unroll
          for (int nt = 0; nt < 5; ++nt) wl2[ks][nt] = wfrag(wb, NPRE + ks, nt);
        SB();
        u32 mx = 0;
#pragma unroll
        for (int ks = 0; ks < 10; ++ks) {
#pragma unroll
          for (int nt = 0; nt < 5; ++nt) {
            res[nt] = __builtin_amdgcn_mfma_f32_16x16x32_bf16(ks < NPRE ? wf[ks < NPRE ? ks : 0][nt] : wl2[ks & 1][nt], __builtin_bit_cast(bf16x8, xf[ks]),
                                                              ks ? res[nt] : acc[nt], 0, 0, 0);
            if (ks >= NPRE && ks < 8) wl2[ks & 1][nt] = wfrag(wb, ks + 2, nt);
          }
          mx = umax_(umax_(umax_(umax_(mx, xf[ks][0]), xf[ks][1]), xf[ks][2]), xf[ks][3]);
          SB();
        }
        if (__all(mx != ~0u)) break;
      }
      lds_arrive(CNT(C_A3));
      STAMP(11);
#pragma unroll
      for (int nt = 0; nt < 5; ++nt) acc[nt] = res[nt];
      STAMP(27);
    }
    const u32 stg = L_W + w * 4096;
#pragma unroll
    for (int i = 0; i < 5; ++i) *lds<u32x2>(stg + l15 * 160 + i * 32 + gq * 8) = (u32x2){pk2(acc[i][0], acc[i][1]), pk2(acc[i][2], acc[i][3])};
    lgkm0();
    const u32 ob = (64 * g + 16 * w) * 2560 + 1280 + hq * 160;
#pragma unroll
    for (int i = 0; i < 3; ++i) {
      const u32 c = i < 2 ? i * 64 + lane : 128 + l31, r = c / 10;
      __builtin_amdgcn_raw_buffer_store_b128(*lds<u32x4>(stg + c * 16), rsOut, (i == 2 && h) ? OOB : ob + r * 2560 + (c - r * 10) * 16, 0, FAUX);
    }
  }
  STAMP(12);
  STAMP_OUT;
}

extern "C" void kernel_launch(void* const* d_in, const int* in_sizes, int n_in, void* d_out, int out_size, void* d_ws, size_t ws_size,
                              hipStream_t stream) {
  fused_kernel<<<256, 512, 0, stream>>>((const u16*)d_in[6], (const u16*)d_in[8], (const u16*)d_in[9], (const u16*)d_in[7], (char*)d_ws,
                                        (const u16*)d_in[1], (const u16*)d_in[2], (const u16*)d_in[3], (const u16*)d_in[4],
                                        (const u16*)d_in[0], (const u16*)d_in[5], (const u16*)d_in[10], (u16*)d_out);
}
```

```cpp
#include <hip/hip_runtime.h>
#include <hip/hip_bf16.h>

typedef unsigned short u16;
typedef unsigned int u32;
typedef unsigned long long u64;
typedef __bf16 bf16x2 __attribute__((ext_vector_type(2)));
typedef __bf16 bf16x8 __attribute__((ext_vector_type(8)));
typedef __bf16 bf16x16 __attribute__((ext_vector_type(16)));
typedef short s16x4 __attribute__((ext_vector_type(4)));
typedef float f32x2 __attribute__((ext_vector_type(2)));
typedef float f32x4 __attribute__((ext_vector_type(4)));
typedef float f32x8 __attribute__((ext_vector_type(8)));
typedef float f32x16 __attribute__((ext_vector_type(16)));
typedef u32 u32x2 __attribute__((ext_vector_type(2)));
typedef u32 u32x4 __attribute__((ext_vector_type(4)));
typedef __amdgpu_buffer_rsrc_t rsrc_t;
#define LAS __attribute__((address_space(3)))
#define GAS __attribute__((address_space(1)))
#define DI __device__ __forceinline__
#define SC1 16
#define NT 2
#define OOB 0x80000000u
#define SB() __builtin_amdgcn_sched_barrier(0)

constexpr int S = 2048, D = 640;
constexpr int NREP = 8;
constexpr u32 WS_E = 0;
constexpr u32 WS_FKV = 4096;
constexpr u32 FSA = 8, FSO = 8;
constexpr u32 WS_FA1 = WS_FKV + NREP * 2048;
constexpr u32 WS_FO = WS_FA1 + 8192;
constexpr u32 WS_REC = 131072;
constexpr u32 WS_KVT = 524288;
constexpr u32 HS = 327680 + 128, GS = 81920 + 128;
constexpr u32 WS_YT = WS_KVT + 8 * HS;
constexpr u32 WS_O = WS_YT + 8 * HS;
constexpr u32 WS_END = WS_O + 32 * GS;
constexpr u32 L_X = 0;
constexpr u32 L_RING = 84480;
constexpr u32 L_WK = L_RING + 40960;
constexpr u32 L_PART = L_WK + 25600;
constexpr u32 L_PAR = L_PART + 8192;
constexpr u32 L_QF = 0;
constexpr u32 L_KV = 12288;
constexpr u32 L_A1 = 34816;
constexpr u32 L_OX = 49152;
constexpr u32 L_WM = 45056;
constexpr u32 L_QT = 86016;
constexpr u32 L_CRED = 12288;
constexpr u32 L_W = 0;
constexpr u32 CV_CH = 21248;
constexpr u32 L_CNT = 163840 - 128;
constexpr u32 L_CV = L_CNT - 3 * CV_CH;
constexpr u32 L_OY = 102400;
constexpr u32 L_GO = L_OY + 36864;
constexpr u32 L_TAG = L_CNT + 120;
constexpr u32 L_END = 163840;
static_assert(L_KV + 22080 <= L_A1 && L_GO + 22528 <= L_CNT && L_PART + 8192 <= L_CNT, "lds");
enum { C_PART, C_KT, C_CS, C_CH, C_P0, C_P1, C_CD, C_A3, C_AT0, C_AT1, C_OR, C_PUB, C_GO, C_K0, C_K1, C_WM, C_SW, C_N };
#define CNT(i) (L_CNT + 4 * (i))

DI rsrc_t mkrs(const void* p, u32 bytes) { return __builtin_amdgcn_make_buffer_rsrc((void*)p, (short)0, (int)bytes, 0x00020000); }
DI u32 pk2(float a, float b) { f32x2 v = {a, b}; return __builtin_bit_cast(u32, __builtin_convertvector(v, bf16x2)); }
DI float lo2f(u32 x) { return __builtin_bit_cast(float, x << 16); }
DI float hi2f(u32 x) { return __builtin_bit_cast(float, x & 0xffff0000u); }
DI float asf(u32 x) { return __builtin_bit_cast(float, x); }
DI u32 asu(float x) { return __builtin_bit_cast(u32, x); }
DI float upper_half(float x) { asm volatile("s_nop 1" : "+v"(x)); const auto r = __builtin_amdgcn_permlane32_swap(asu(x), asu(x), false, false); return asf(r[1]); }
DI u32 umin_(u32 a, u32 b) { return a < b ? a : b; }
DI u32 umax_(u32 a, u32 b) { return a > b ? a : b; }
DI float add1(float a, float b) { float r; asm("v_add_f32 %0, %1, %2" : "=v"(r) : "v"(a), "v"(b)); return r; }
DI float mul1(float a, float b) { float r; asm("v_mul_f32 %0, %1, %2" : "=v"(r) : "v"(a), "v"(b)); return r; }
DI f32x4 add4(f32x4 a, f32x4 b) { return (f32x4){add1(a[0], b[0]), add1(a[1], b[1]), add1(a[2], b[2]), add1(a[3], b[3])}; }
DI u32 pk2s(float a, float b) { return umin_(pk2(a, b), 0xFFFFFFFEu); }
DI void lgkm0() { asm volatile("s_waitcnt lgkmcnt(0)" ::: "memory"); }
DI void vm0() { asm volatile("s_waitcnt vmcnt(0)" ::: "memory"); }
template <int N> DI void vmw() { asm volatile("s_waitcnt vmcnt(%0)" ::"n"(N) : "memory"); }
DI void bar() { asm volatile("s_waitcnt lgkmcnt(0)\n\ts_barrier" ::: "memory"); }
template <class T> DI LAS T* lds(u32 off) { return (LAS T*)(size_t)off; }
DI s16x4 trread(u32 addr) { return __builtin_bit_cast(s16x4, __builtin_amdgcn_ds_read_tr16_b64_v4i16((LAS s16x4*)(size_t)addr)); }
DI bf16x8 cat8(s16x4 a, s16x4 b) { return __builtin_bit_cast(bf16x8, __builtin_shufflevector(a, b, 0, 1, 2, 3, 4, 5, 6, 7)); }
DI void dma16(u32 lds_dst, u32 voff, rsrc_t rs, u32 soff) {
  u32 keep;
  asm volatile("s_mov_b32 %0, m0\n\ts_mov_b32 m0, %1\n\ts_nop 0\n\tbuffer_load_dwordx4 %2, %3, %4 offen lds\n\ts_mov_b32 m0, %0"
               : "=&s"(keep) : "s"(lds_dst), "v"(voff), "s"(rs), "s"(soff) : "memory");
}
DI void dma16nt(u32 lds_dst, u32 voff, rsrc_t rs, u32 soff) {
  u32 keep;
  asm volatile("s_mov_b32 %0, m0\n\ts_mov_b32 m0, %1\n\ts_nop 0\n\tbuffer_load_dwordx4 %2, %3, %4 offen nt lds\n\ts_mov_b32 m0, %0"
               : "=&s"(keep) : "s"(lds_dst), "v"(voff), "s"(rs), "s"(soff) : "memory");
}
template <int N> struct IC { static constexpr int value = N; };
template <int N, int I = 0, class F> DI void static_for(F f) { if constexpr (I < N) { f(IC<I>{}); static_for<N, I + 1>(f); } }
template <int OFF> DI void d16hi(float& x, u32 addr) { asm volatile("ds_read_u16_d16_hi %0, %1 offset:%2" : "+v"(x) : "v"(addr), "n"(OFF) : "memory"); }
DI u32 swap23(u32 x) { return (x & ~12u) | ((x & 4u) << 1) | ((x & 8u) >> 1); }
DI int mod5(int x) { return x >= 5 ? x - 5 : x; }

constexpr u32 SPIN_CAP = 1u << 17;
DI bool poll_flags(const char* p, bool active, u64 tag) {
  u64 v = tag;
  if (active) v = __hip_atomic_load((GAS u64*)p, __ATOMIC_RELAXED, __HIP_MEMORY_SCOPE_AGENT);
  return __all(v == tag);
}
template <int SLP = 1>
DI void wait_flags(const char* p, bool active, u64 tag) {
  if (__builtin_expect(!poll_flags(p, active, tag), 0)) {
#pragma unroll 1
    for (u32 spins = 0; spins < SPIN_CAP; ++spins) {
      __builtin_amdgcn_s_sleep(SLP);
      if (poll_flags(p, active, tag)) break;
    }
  }
  asm volatile("" ::: "memory");
}
DI void put_flag(char* p, u64 tag) { __hip_atomic_store((GAS u64*)p, tag, __ATOMIC_RELAXED, __HIP_MEMORY_SCOPE_AGENT); }
DI void lds_arrive(u32 cnt) {
  asm volatile("s_waitcnt lgkmcnt(0)\n\ts_mov_b64 exec, 1\n\tds_add_u32 %0, %1\n\ts_mov_b64 exec, -1" ::"v"(cnt), "v"(1u) : "memory");
}
template <int SLP = 0>
DI void lds_wait(u32 cnt, u32 target) {
  u32 v, sv, n = SPIN_CAP << 7;
  asm volatile("1:\n\tds_read_b32 %0, %3\n\ts_sub_u32 %2, %2, 1\n\ts_waitcnt lgkmcnt(0)\n\tv_readfirstlane_b32 %1, %0\n\t"
               "s_cselect_b32 %1, -1, %1\n\ts_cmp_lt_u32 %1, %4\n\ts_cbranch_scc0 2f\n\ts_sleep %5\n\ts_branch 1b\n2:"
               : "=&v"(v), "=&s"(sv), "+s"(n) : "v"(cnt), "s"(target), "n"(SLP) : "memory", "scc");
}

template <int NA> struct CFrag { u32x4 a[NA], b[NA]; };
template <int NA> DI void conv_load(CFrag<NA>& f, u32 hb, u32 xb) {
#pragma unroll
  for (int a = 0; a < NA; ++a) {
    f.b[NA - 1 - a] = *(volatile LAS u32x4*)(size_t)(xb + 512 * (8 - NA + a));
    const u32x2 p = *(volatile LAS u32x2*)(size_t)(hb + 512 * a), q = *(volatile LAS u32x2*)(size_t)(hb + 512 * a + 8);
    f.a[a] = (u32x4){p[0], p[1], q[0], q[1]};
  }
}
template <int MASK, int NA> DI void conv_mma(const CFrag<NA>& f, f32x4 (&acc)[8]) {
#pragma unroll
  for (int a = 0; a < NA; ++a)
#pragma unroll
    for (int c = NA - 1; c >= 0; --c)
      if (a + c < 8 && ((MASK >> (a + c)) & 1))
        acc[a + c] = __builtin_amdgcn_mfma_f32_16x16x32_bf16(__builtin_bit_cast(bf16x8, f.a[a]), __builtin_bit_cast(bf16x8, f.b[c]), acc[a + c], 0, 0, 0);
}
template <int NM, int ND> DI void weave() {
#pragma unroll
  for (int i = 0; i < NM; ++i) {
    __builtin_amdgcn_sched_group_barrier(0x8, 1, 0);
    __builtin_amdgcn_sched_group_barrier(0x100, ND, 0);
  }
  SB();
}
constexpr int tri_0(int a) { return a * 8 - a * (a - 1) / 2; }
constexpr int tri_a(int n) { int a = 0; while (tri_0(a + 1) <= n) ++a; return a; }
DI u32 conv_xb(u32 img, int lane) { return img + 16640 + 480 - 32 * (lane & 15) + 16 * (lane >> 4); }
DI u32 conv_hb(u32 img, int lane) { const u32 e = (lane & 15) + 1 + 8 * (lane >> 4); return img + (e & 3) * 4160 + (e & ~3u) * 2; }
template <int MASK, int NA>
DI void conv_shared(u32 img, int wq4, int lane) {
  const u32 xb = conv_xb(img, lane) + wq4 * 128, hb = conv_hb(img, lane) + wq4 * 128;
  const u32 dst = L_CRED + wq4 * 5120 + (lane & 15) * 64 + (lane >> 4) * 16;
  f32x4 acc[8];
#pragma unroll
  for (int i = 0; i < 8; ++i) acc[i] = (f32x4){0.f, 0.f, 0.f, 0.f};
  CFrag<NA> f0, f1;
  conv_load(f0, hb, xb);
  SB();
  conv_load(f1, hb + 64, xb + 64);
  conv_mma<MASK>(f0, acc);
  weave<12, 2>();
  conv_mma<MASK>(f1, acc);
  SB();
  int n = 0;
#pragma unroll
  for (int i = 0; i < 8; ++i)
    if ((MASK >> i) & 1) *lds<f32x4>(dst + 1024 * n++) = acc[i];
}

struct Rec { u32x4 k1a, k1b, v0, v1, v2, v3; u32x2 k2a, k2b; };
#define LD128(off, so) __builtin_amdgcn_raw_buffer_load_b128(rsW, v16 + (off), so, SC1)
#define LDV(off, so) __builtin_amdgcn_raw_buffer_load_b128(rsW, vv + (off), so, SC1)
#define LD64(off, so) __builtin_amdgcn_raw_buffer_load_b64(rsW, v8 + (off), so, SC1)
#define LOAD_H1(R, so) asm volatile( \
    "buffer_load_dwordx4 %0, %4, %7, %8 offen sc1\n\tbuffer_load_dwordx2 %1, %5, %7, %8 offen offset:2048 sc1\n\t" \
    "buffer_load_dwordx4 %2, %6, %7, %8 offen offset:3072 sc1\n\tbuffer_load_dwordx4 %3, %6, %7, %9 offen sc1" \
    : "=&v"(R.k1a), "=&v"(R.k2a), "=&v"(R.v0), "=&v"(R.v1) : "v"(v16), "v"(v8), "v"(vv), "s"(rsW), "s"(so), "s"((so) + 4096) : "memory")
#define LOAD_H2(R, so) asm volatile( \
    "buffer_load_dwordx4 %0, %4, %7, %8 offen offset:1024 sc1\n\tbuffer_load_dwordx2 %1, %5, %7, %8 offen offset:2560 sc1\n\t" \
    "buffer_load_dwordx4 %2, %6, %7, %9 offen offset:1024 sc1\n\tbuffer_load_dwordx4 %3, %6, %7, %9 offen offset:2048 sc1" \
    : "=&v"(R.k1b), "=&v"(R.k2b), "=&v"(R.v2), "=&v"(R.v3) : "v"(v16), "v"(v8), "v"(vv), "s"(rsW), "s"(so), "s"((so) + 4096) : "memory")
#define WAIT_H1(R, n) asm volatile("s_waitcnt vmcnt(" #n ")" : "+v"(R.k1a), "+v"(R.k2a), "+v"(R.v0), "+v"(R.v1)::"memory")
#define WAIT_H2(R, n) asm volatile("s_waitcnt vmcnt(" #n ")" : "+v"(R.k1b), "+v"(R.k2b), "+v"(R.v2), "+v"(R.v3)::"memory")
DI f32x16 qk16(u32x4 k1, u32x4 q1) {
  f32x16 z;
#pragma unroll
  for (int i = 0; i < 16; ++i) z[i] = 0.f;
  return __builtin_amdgcn_mfma_f32_32x32x16_bf16(__builtin_bit_cast(bf16x8, k1), __builtin_bit_cast(bf16x8, q1), z, 0, 0, 0);
}
DI f32x16 qk8(u32x2 k2, u32x2 q2, f32x16 s) {
  return __builtin_amdgcn_mfma_f32_32x32x8bf16_1k(__builtin_bit_cast(s16x4, k2), __builtin_bit_cast(s16x4, q2), s, 0, 0, 0);
}
DI f32x16 pv_lo(u32x4 v, f32x16 p, f32x16 o, int idx) {
  f32x8 h = __builtin_shufflevector(p, p, 0, 1, 2, 3, 4, 5, 6, 7);
  return __builtin_amdgcn_smfmac_f32_32x32x32_bf16(__builtin_bit_cast(bf16x8, v), __builtin_bit_cast(bf16x16, h), o, idx, 0, 0);
}
DI f32x16 pv_hi(u32x4 v, f32x16 p, f32x16 o, int idx) {
  f32x8 h = __builtin_shufflevector(p, p, 8, 9, 10, 11, 12, 13, 14, 15);
  return __builtin_amdgcn_smfmac_f32_32x32x32_bf16(__builtin_bit_cast(bf16x8, v), __builtin_bit_cast(bf16x16, h), o, idx, 0, 0);
}
#define EXP4(X, i) do { X[i] = __builtin_amdgcn_exp2f(X[i]); X[i + 1] = __builtin_amdgcn_exp2f(X[i + 1]); X[i + 2] = __builtin_amdgcn_exp2f(X[i + 2]); X[i + 3] = __builtin_amdgcn_exp2f(X[i + 3]); } while (0)
#define VMWAIT(n) __builtin_amdgcn_s_waitcnt(0x0F70 | ((n) & 15) | (((n) >> 4) << 14))
#define STEPX(SI, H, HP, HN, VPREV, VCUR, K1N, K2N, QK, MID, END) do { \
    EXP4(Sx[SI], 0); SB(); \
    if ((QK) != 2) O[HP] = pv_hi(VPREV, Sx[(SI + 3) & 3], O[HP], idx); \
    SB(); MID; SB(); \
    EXP4(Sx[SI], 4); SB(); \
    if (QK) Sx[(SI + 2) & 3] = qk16(K1N, q1[HN]); \
    SB(); \
    EXP4(Sx[SI], 8); SB(); \
    if (QK) Sx[(SI + 2) & 3] = qk8(K2N, q2[HN], Sx[(SI + 2) & 3]); \
    SB(); \
    EXP4(Sx[SI], 12); SB(); \
    O[H] = pv_lo(VCUR, Sx[SI], O[H], idx); SB(); \
    END; SB(); \
  } while (0)
#define STEP(H, VPREV, VCUR, K1N, K2N, QK, WAITN, REFILL) \
    STEPX(H, H, (H + 3) & 3, (H + 2) & 3, VPREV, VCUR, K1N, K2N, QK, , if ((WAITN) >= 0) VMWAIT((WAITN) >= 0 ? (WAITN) : 0); REFILL)
#define WN(r, i) ((r) < 6 ? 10 : 10 - (i))
#define REC_STEPS(R, N, r) do { \
    const u32 sn = rec0 + ((r) + 1) * 7168 + 4096, so = sn + 3072, sp = so + 4096; \
    STEP(0, N.v3, R.v0, R.k1a, R.k2a, (r) == 0 ? 2 : 1, (r) == 0 ? -1 : WN(r, 0), if ((r) > 0) N.v3 = LDV(2048, sn)); \
    STEP(1, R.v1, R.v0, R.k1a, R.k2a, 1, (r) < 2 ? -1 : WN(r, 1), if ((r) < 2) WAIT_H2(R, 8); if ((r) < 6) R.k1a = LD128(0, so)); \
    STEP(2, R.v1, R.v0, R.k1b, R.k2b, 1, -1, if ((r) < 6) R.k2a = LD64(2048, so)); \
    STEP(3, R.v1, R.v0, R.k1b, R.k2b, 1, (r) == 0 ? -1 : WN(r, 3), if ((r) < 6) R.v0 = LDV(3072, so)); \
    STEP(0, R.v1, R.v2, R.k1b, R.k2b, 1, (r) == 0 ? -1 : WN(r, 4), if ((r) < 6) R.v1 = LDV(0, sp)); \
    STEP(1, R.v3, R.v2, R.k1b, R.k2b, 1, (r) == 0 ? -1 : WN(r, 5), if ((r) == 0) WAIT_H1(N, 8); if ((r) < 6) R.k1b = LD128(1024, so)); \
    STEP(2, R.v3, R.v2, N.k1a, N.k2a, 1, (r) < 6 ? -1 : 2, if ((r) < 6) R.k2b = LD64(2560, so)); \
    if ((r) < 6) STEP(3, R.v3, R.v2, N.k1a, N.k2a, 1, WN(r, 7), R.v2 = LDV(1024, sp)); \
    else STEPX(3, 3, 2, 0, R.v3, R.v2, N.k1b, N.k2b, 1, , VMWAIT(0)); \
  } while (0)

__global__ void __launch_bounds__(512, 2)
fused_kernel(const u16* __restrict__ Bx, const u16* __restrict__ Bwk, const u16* __restrict__ Bwv, const u16* __restrict__ Bwq, char* ws,
             const u16* __restrict__ Ak, const u16* __restrict__ Av, const u16* __restrict__ Ash, const u16* __restrict__ Assm,
             const u16* __restrict__ Aq, const u16* __restrict__ AD, const u16* __restrict__ Bwo, u16* __restrict__ out) {
  __shared__ __attribute__((aligned(16))) char smem[L_END];
  asm volatile("" ::"v"((u32)(size_t)(LAS char*)smem));
  const int lane = threadIdx.x & 63;
  const int w = __builtin_amdgcn_readfirstlane(threadIdx.x >> 6);
  const int bj = blockIdx.x >> 3, g = (blockIdx.x & 7) * 4 + (bj >> 3), hq = bj & 7;
  const int gq = lane >> 4, l15 = lane & 15;
  const int half = w >> 2, wq4 = w & 3, h = lane >> 5, l31 = lane & 31;
  const int hp = (hq >> 1) & 1;
  const rsrc_t rsW = mkrs(ws, WS_END), rsX = mkrs(Bx, S * D * 2), rsQ = mkrs(Bwq, D * D * 2);
  const rsrc_t rsK = mkrs((hq & 1) ? Bwv : Bwk, D * 20 * 2);
  asm volatile("s_nop 4" ::"s"(rsW), "s"(rsX), "s"(rsQ), "s"(rsK), "s"(Ak), "s"(Av), "s"(Ash), "s"(Assm), "s"(Aq), "s"(AD), "s"(Bwo), "s"(out));

  u32x4 q1[4];
  u32x2 q2[4];
  u64 tag;
  Rec A, B;
  f32x16 O[4];
  u32 vm0_, vm1_, vt0, vt1, vt2;
  auto zero_o = [&]() {
#pragma unroll
    for (int i = 0; i < 4; ++i) {
#pragma unroll
      for (int j = 0; j < 16; ++j) O[i][j] = 0.f;
      asm volatile("" : "+v"(O[i]));
    }
  };
  const u32 v16 = lane * 16, vv = (h * 32 + umin_(l31, 23)) * 16, v8 = umin_(lane, 32) * 8, rec0 = WS_REC + wq4 * 8 * 7168;
  const u32 r6 = lane / 10, vo6 = r6 * 1280 + (lane - r6 * 10) * 16 + hq * 160;
  auto tile64 = [&](u32 dst, const u16* src) {
    const rsrc_t rs = mkrs(src, S * D * 2);
    const int r0 = 64 * g + 18 * wq4;
    if (lane < 60) {
      dma16nt(dst + wq4 * 2880, vo6, rs, r0 * 1280);
      dma16nt(dst + wq4 * 2880 + 960, (wq4 == 3 && r6 > 3) ? OOB : vo6, rs, (r0 + 6) * 1280);
      if (wq4 < 3) dma16nt(dst + wq4 * 2880 + 1920, vo6, rs, (r0 + 12) * 1280);
    }
  };
  const char* kvflag = ws + WS_FKV + (blockIdx.x & (NREP - 1)) * 2048 + (wq4 * 64 + lane) * 8;
  const int rot = g & 3;
  const int rbk = hq >> 1;
  const u32 xvo = gq * 1280 + ((l15 ^ (4 * gq)) * 16);
  auto wsrc = [&](int ph) {
    const u32 c = 64 * ph + lane, p = c / 6;
    return swap23(p) * 1280 + (c - p * 6) * 16;
  };
  auto dma_w = [&](int s, int blk, int ph, u32 vo) {
    dma16(L_RING + s * 12288 + blk * 3072 + ph * 1024, vo, rsQ, (mod5(rot + s) * 128 + blk * 32) * 1280 + hq * 160 + hp * 64);
  };
  auto dma_x = [&](int s, int idx) {
    const int j = mod5(rot + s), m = (4 * rbk + 4 + idx) & 15;
    dma16(L_X + (j * 16 + m) * 1056, xvo, rsX, (64 * g + 4 * m) * 1280 + 256 * j);
  };
  auto qaddr = [&](int tile) {
    const u32 G = 4 * tile + gq, hd = G / 5, ga = G - 5 * hd, q = 16 * (wq4 & 1) + l15, slot = (hd & 1) + (wq4 & 2);
    return L_QF + ((hd >> 1) ^ hp) * 6144 + (ga < 4 ? slot * 1024 + (q + 32 * (ga & 1)) * 16 + (ga >> 1) * 8 : 4096 + slot * 512 + q * 8);
  };
  auto read_q = [&]() {
#pragma unroll
    for (int i = 0; i < 4; ++i) {
      const u32 sl = (wq4 + 1 + i) & 3;
      q1[i] = *lds<u32x4>(L_QF + half * 6144 + sl * 1024 + lane * 16);
      q2[i] = *lds<u32x2>(L_QF + half * 6144 + 4096 + sl * 512 + lane * 8);
    }
  };
  u32 xa[4], wv0, wv1, wv2;
#pragma unroll
  for (int i = 0; i < 4; ++i) xa[i] = L_X + (4 * wq4 + (l15 >> 2)) * 1056 + (l15 & 3) * 256 + gq * 16 + ((i ^ (l15 & 3)) * 64);
  {
    if (w == 0) {
      if (lane < C_N) *lds<u32>(CNT(lane)) = 0;
      lgkm0();
    }
    if (w >= 3) {
      const int j = w - 3;
#pragma unroll
      for (int m = 0; m < 4; ++m) dma16nt(L_X + (j * 16 + 4 * rbk + m) * 1056, xvo, rsX, (64 * g + 16 * rbk + 4 * m) * 1280 + 256 * j);
      const int i0 = (bj >> 1) % 5;
#pragma unroll
      for (int i = 0; i < 5; ++i) { const u32 o = j * 5120 + mod5(i0 + i) * 1024; dma16(L_WK + o, lane * 16, rsK, o); }
    } else {
      if (w == 1 && lane < 40) dma16(L_PAR, lane * 16, mkrs(Ash, D * 4 * 2), hq * 640);
      if (w == 2 && lane < 10) dma16(L_PAR + 768, lane * 16, mkrs(AD, D * 2), hq * 160);
    }
    u64 ep, pc;
    u32 pfs, len;
    asm volatile("s_getpc_b64 %0\n.Lpf_pc%=:\n\ts_mov_b32 %1, .Lpf_end-.Lpf_pc%=" : "=s"(pc), "=s"(len));
    pc += umin_((u32)(bj * 8 + w) * 192, len - 0x88);
    asm volatile("s_load_dwordx2 %0, %2, 0x0\n\ts_load_dword %1, %3, 0x0\n\ts_load_dword %1, %3, 0x40\n\ts_load_dword %1, %3, 0x80"
                 : "=&s"(ep), "=&s"(pfs) : "s"(w == 7 ? (u64)(ws + WS_E) : pc), "s"(pc) : "memory");
    u32 wv = wsrc(w);
    asm volatile("s_barrier" : "+v"(wv)::"memory");
    if (w < 3) {
#pragma unroll
      for (int i = 0; i < 4; ++i) dma_w(0, i, w, wv);
#pragma unroll
      for (int i = 0; i < 4; ++i) dma_x(0, 4 * w + i);
#pragma unroll
      for (int i = 0; i < 4; ++i) dma_x(1, 4 * w + i);
#pragma unroll
      for (int i = 0; i < 4; ++i) dma_w(1, i, w, wv);
#pragma unroll
      for (int i = 0; i < 4; ++i) dma_x(2, 4 * w + i);
    } else {
      const int j = w - 3;
      u32 rb = L_WK + j * 5120 + (8 * gq + ((lane >> 2) & 3)) * 40 + 8 * (lane & 3);
      u32 xr[4], pa = L_PART + j * 2048 + lane * 16;
#pragma unroll
      for (int ks = 0; ks < 4; ++ks) xr[ks] = L_X + (j * 16 + 4 * rbk + (l15 >> 2)) * 1056 + (l15 & 3) * 256 + gq * 16 + ((ks ^ (l15 & 3)) * 64);
      const u32 rec = WS_REC + g * 7168, T = rbk >> 1, key = 16 * (rbk & 1) + l15 + 32 * gq;
      u32 sa0 = (hq & 1) ? rec + 3072 + rbk * 1024 + (gq >> 1) * 512 + ((gq & 1) ? 16 + l15 : swap23(l15)) * 16 : (gq < 2 ? rec + T * 1024 + key * 16 : OOB);
      u32 sa1 = gq < 2 ? rec + 2048 + T * 512 + key * 8 : OOB;
      wv0 = wsrc(0); wv1 = wsrc(1); wv2 = wsrc(2);
      asm volatile("s_waitcnt vmcnt(0) lgkmcnt(0)"
                   : "+v"(rb), "+v"(xr[0]), "+v"(xr[1]), "+v"(xr[2]), "+v"(xr[3]), "+v"(pa), "+v"(sa0), "+v"(sa1), "+v"(wv0), "+v"(wv1), "+v"(wv2)::"memory");
      f32x4 a0 = {0.f, 0.f, 0.f, 0.f}, a1v = a0;
      bf16x8 w0[4], w1[4], xk[4];
#pragma unroll
      for (int ks = 0; ks < 4; ++ks) {
        xk[ks] = *lds<bf16x8>(xr[ks]);
        w0[ks] = cat8(trread(rb + ks * 1280), trread(rb + ks * 1280 + 160));
        w1[ks] = cat8(trread(rb + ks * 1280 + 32), trread(rb + ks * 1280 + 192));
      }
      SB();
      if (hq & 1) {
#pragma unroll
        for (int ks = 0; ks < 4; ++ks) {
          a0 = __builtin_amdgcn_mfma_f32_16x16x32_bf16(xk[ks], w0[ks], a0, 0, 0, 0);
          a1v = __builtin_amdgcn_mfma_f32_16x16x32_bf16(xk[ks], w1[ks], a1v, 0, 0, 0);
        }
      } else {
#pragma unroll
        for (int ks = 0; ks < 4; ++ks) {
          a0 = __builtin_amdgcn_mfma_f32_16x16x32_bf16(w0[ks], xk[ks], a0, 0, 0, 0);
          a1v = __builtin_amdgcn_mfma_f32_16x16x32_bf16(w1[ks], xk[ks], a1v, 0, 0, 0);
        }
      }
      if (__builtin_expect(w < 7, 1)) {
        *lds<f32x4>(pa) = a0;
        *lds<f32x4>(pa + 1024) = a1v;
        lds_arrive(CNT(C_PART));
      }
      asm volatile("" ::: "memory");
      if (__builtin_expect(w == 7, 1)) {
        __builtin_amdgcn_s_setprio(3);
        u32 pb = pa - 8192;
        asm volatile("" : "+v"(pb));
        lds_wait(CNT(C_PART), 4);
        f32x4 p0[4], p1[4];
#pragma unroll
        for (int i = 0; i < 4; ++i) {
          p0[i] = *lds<f32x4>(pb + i * 2048);
          p1[i] = *lds<f32x4>(pb + i * 2048 + 1024);
        }
        SB();
        a0 += (p0[0] + p0[1]) + (p0[2] + p0[3]);
        a1v += (p1[0] + p1[1]) + (p1[2] + p1[3]);
        u32x2 o0 = {pk2(a0[0], a0[1]), pk2(a0[2], a0[3])}, o1 = {pk2(a1v[0], a1v[1]), pk2(a1v[2], a1v[3])};
        if (hq & 1) {
          if (l15 == 4) o1 = (u32x2){0x3f803f80u, 0x3f803f80u};
          if (l15 > 4) o1 = (u32x2){0u, 0u};
          const auto x = __builtin_amdgcn_permlane16_swap(o0[0], o1[0], false, false);
          const auto y = __builtin_amdgcn_permlane16_swap(o0[1], o1[1], false, false);
          __builtin_amdgcn_raw_buffer_store_b128((u32x4){x[0], y[0], x[1], y[1]}, rsW, sa0, 0, SC1);
        } else {
          const float c = 0.22360679774997896f * 1.4426950408889634f;
          o0 = (u32x2){pk2(lo2f(o0[0]) * c, hi2f(o0[0]) * c), pk2(lo2f(o0[1]) * c, hi2f(o0[1]) * c)};
          o1 = (u32x2){pk2(lo2f(o1[0]) * c, hi2f(o1[0]) * c), pk2(lo2f(o1[1]) * c, hi2f(o1[1]) * c)};
          if (gq == 1) o1 = (u32x2){0x3f803f80u, 0x00003f80u};
          const auto x = __builtin_amdgcn_permlane32_swap(o0[0], o0[0], false, false);
          const auto y = __builtin_amdgcn_permlane32_swap(o0[1], o0[1], false, false);
          __builtin_amdgcn_raw_buffer_store_b128((u32x4){x[0], y[0], x[1], y[1]}, rsW, sa0, 0, SC1);
          __builtin_amdgcn_raw_buffer_store_b64(o1, rsW, sa1, 0, SC1);
        }
      }
    }
    asm volatile("s_waitcnt lgkmcnt(0)" : "+s"(ep) : "s"(pfs) : "memory");
    tag = ep + 0x9E3779B97F4A7C15ull;
  }
  auto get_tag = [&]() {
    const u32x2 tv = *lds<u32x2>(L_TAG);
    tag = (u64)(u32)__builtin_amdgcn_readfirstlane(tv[0]) | ((u64)(u32)__builtin_amdgcn_readfirstlane(tv[1]) << 32);
  };
  const u32 oo = WS_O + g * GS + (((wq4 & 2) + (l31 >> 4)) * 20 + 10 * half) * 1024 + l15 * 16;
  u32 oa0 = oo + hq * 1024 + (2 * (wq4 & 1) + h) * 256, oa1 = h ? OOB : oo + (8 + (hq >> 2)) * 1024 + (hq & 3) * 256 + (wq4 & 1) * 8;
  auto put_o = [&](u32x4 o0, u32x2 o1) {
    __builtin_amdgcn_raw_buffer_store_b128(o0, rsW, oa0, 0, SC1);
    __builtin_amdgcn_raw_buffer_store_b64(o1, rsW, oa1, 0, SC1);
  };
  auto chan = [&](int c) { return 5 * (g >> 1) + (c == 0 ? 2 : 3 * (g & 1) + c - 1); };
  if (__builtin_expect(half == 1, 0)) {
    bf16x8 xy[5][4];
    u32x4 tm[3][2];
    u32 dd[3];
    float vf[3][8];
    const u32 xyb = L_X + (l15 >> 2) * 1056 + (l15 & 3) * 256 + gq * 16 + ((wq4 ^ (l15 & 3)) * 64);
    const u32 mv = (lane >> 2) * 1280 + ((lane & 3) ^ (2 * h)) * 16;
    auto mdma = [&](int i) {
      dma16(L_WM + wq4 * 4096 + (i & 3) * 1024, mv, rsQ, (mod5(rot + (i >> 1)) * 128 + 32 * wq4 + 16 * (i & 1)) * 1280 + hq * 160 + (hp ? 0 : 96));
    };
    u32 qa0 = qaddr(hp ? 0 : 3), qa1 = qaddr(hp ? 1 : 4), wm0 = L_WM + wq4 * 4096 + (8 * gq + ((lane >> 2) & 3)) * 64 + 8 * (lane & 3) + (gq & 1) * 32, wm1 = wm0 ^ 32;
    f32x4 ma[4][2];
    auto mstage = [&](int st) {
      const bf16x8 w0 = cat8(trread(wm0 + (st & 1) * 2048), trread(wm0 + (st & 1) * 2048 + 256));
      const bf16x8 w1 = cat8(trread(wm1 + (st & 1) * 2048), trread(wm1 + (st & 1) * 2048 + 256));
      SB();
#pragma unroll
      for (int i = 0; i < 4; ++i) {
        ma[i][0] = __builtin_amdgcn_mfma_f32_16x16x32_bf16(w0, xy[st][i], st ? ma[i][0] : (f32x4){0.f, 0.f, 0.f, 0.f}, 0, 0, 0);
        ma[i][1] = __builtin_amdgcn_mfma_f32_16x16x32_bf16(w1, xy[st][i], st ? ma[i][1] : (f32x4){0.f, 0.f, 0.f, 0.f}, 0, 0, 0);
      }
      SB();
    };
    {
      auto duty = [&](int b) {
        dma_w(b, wq4, 0, wv0);
        dma_w(b, wq4, 1, wv1);
        dma_w(b, wq4, 2, wv2);
        if (b < 4) {
#pragma unroll
          for (int i = 0; i < 3; ++i) dma_x(b + 1, 3 * wq4 + i);
        }
      };
      auto rdx = [&](int s) {
#pragma unroll
        for (int i = 0; i < 4; ++i) xy[s][i] = *lds<bf16x8>(xyb + ((wq4 + 1 + i) & 3) * 4224 + mod5(rot + s) * 16896);
      };
      auto zq = [&]() {
        *lds<u32x2>(L_QF + 4096 + wq4 * 512 + lane * 8) = (u32x2){0u, 0u};
        *lds<u32x2>(L_QF + 6144 + 4096 + wq4 * 512 + lane * 8) = (u32x2){0u, 0u};
      };
      auto publish = [&]() {
        if (lane < NREP) put_flag(ws + WS_FKV + lane * 2048 + (g * 8 + hq) * 8, tag);
      };
      if (w == 7) {
        if (lane == 0) *lds<u64>(L_TAG) = tag;
      }
      duty(2);
      if (w == 7) { vmw<6>(); publish(); }
      bar();
      get_tag();
      duty(3);
      rdx(0);
      rdx(1);
      bar();
      rdx(2);
#pragma unroll
      for (int i = 0; i < 3; ++i) {
        const u32 tap = (u32)*lds<u16>(L_PAR + (16 * (wq4 >> 1) + l15) * 8 + i * 256 + (3 - gq) * 2) << (16 * (l15 & 1));
        dd[i] = (u32)*lds<u16>(L_PAR + 768 + (16 * (wq4 >> 1) + l15) * 2 + i * 64) << 16;
#pragma unroll
        for (int e = 0; e < 8; ++e) tm[i][e >> 2][e & 3] = (l15 >> 1) == e ? tap : 0u;
        asm volatile("" : "+v"(tm[i][0]), "+v"(tm[i][1]), "+v"(dd[i]));
#pragma unroll
        for (int j = 0; j < 8; ++j) { vf[i][j] = 0.f; asm volatile("" : "+v"(vf[i][j])); }
      }
      asm volatile("" : "+v"(qa0), "+v"(qa1), "+v"(wm0), "+v"(wm1), "+v"(oa0), "+v"(oa1));
      vmw<6>();
      bar();
      rdx(3);
      duty(4);
      vmw<3>();
      bar();
      rdx(4);
      vm0();
      bar();
      __builtin_amdgcn_s_sleep(2);
      mdma(0);
      mdma(1);
      if (h) zq();
      bar();
    }
    if (lane < 60) {
      const rsrc_t rsAk = mkrs(Ak, S * D * 2);
      const int rk = 64 * g - 3 + 18 * wq4;
      dma16nt(L_KV + wq4 * 2880, rk < 0 ? (r6 < 3 ? OOB : vo6 - 3840) : vo6, rsAk, rk < 0 ? 0 : rk * 1280);
      dma16nt(L_KV + wq4 * 2880 + 960, vo6, rsAk, (rk + 6) * 1280);
      dma16nt(L_KV + wq4 * 2880 + 1920, (wq4 == 3 && r6 > 0) ? OOB : vo6, rsAk, (rk + 12) * 1280);
    }
    __builtin_amdgcn_s_setprio(3);
    __builtin_amdgcn_s_sleep(3);
    tile64(L_KV + 11520, Av);
    vmw<5>();
    mstage(0);
    auto ymark = [&]() {
#pragma unroll
      for (int i = 0; i < 3; ++i)
        if (i < 2 || wq4 < 2)
          __builtin_amdgcn_raw_buffer_store_b128((u32x4){~0u, ~0u, ~0u, ~0u}, rsW,
              WS_YT + hq * HS + (g >> 2) * 40960 + (8 * wq4 + (lane >> 3)) * 512 + (g & 3) * 128 + (lane & 7) * 16 + i * 16384, 0, SC1);
    };
    vm0();
    lds_arrive(CNT(C_KT));
    const rsrc_t rsH = mkrs(Assm, D * S * 2);
    const u32 hrow = wq4 < 3 ? (hq * 80 + chan(wq4)) * 4096 : OOB;
    u32x4 ht[4];
#pragma unroll
    for (int i = 0; i < 4; ++i) ht[i] = __builtin_amdgcn_raw_buffer_load_b128(rsH, lane * 16 + i * 1024, hrow, NT);
    ymark();
    lds_wait<1>(CNT(C_KT), 4);
    {
      const u32 run = 4 * (wq4 & 1) + gq;
      const u32 ka = L_KV + 8 * run * 160 + (wq4 >> 1) * 32 + 2 * l15;
      const u32 sa = L_A1 + (16 * (wq4 >> 1) + l15) * 128 + ((7 - run) ^ (l15 & 7)) * 16;
      const u32 cr = 8 * (wq4 & 1) + (lane >> 3), sl = lane & 7;
      const u32 ra = L_A1 + (16 * (wq4 >> 1) + cr) * 128 + (sl ^ (cr & 7)) * 16;
      const u32 so = WS_KVT + hq * HS + (7 - (g >> 2)) * 40960 + (16 * (wq4 >> 1) + cr) * 512 + (3 - (g & 3)) * 128 + sl * 16;
      u32x4 md[3];
      const u32 kb = L_KV + (32 * (wq4 & 1) + 8 * (l15 >> 2) + (l15 & 3) + gq) * 160 + (wq4 >> 1) * 32;
      f32x4 ks[3][2];
      auto prod = [&](auto I) {
        constexpr int i = decltype(I)::value;
        bf16x8 kr[2][2];
#pragma unroll
        for (int n = 0; n < 4; ++n) kr[n >> 1][n & 1] = *lds<bf16x8>(kb + i * 64 + (n >> 1) * 640 + (n & 1) * 16);
        SB();
#pragma unroll
        for (int e = 0; e < 2; ++e)
#pragma unroll
          for (int T = 0; T < 2; ++T)
            ks[i][T] = __builtin_amdgcn_mfma_f32_16x16x32_bf16(kr[T][e], __builtin_bit_cast(bf16x8, tm[i][e]), e ? ks[i][T] : (f32x4){0.f, 0.f, 0.f, 0.f}, 0, 0, 0);
        static_for<8>([&](auto J) { d16hi<11520 + i * 64 + decltype(J)::value * 160>(vf[i][decltype(J)::value], ka); });
      };
      auto calc = [&](int i) {
        float(&v)[8] = vf[i];
        asm volatile("s_waitcnt lgkmcnt(0)\n\ts_nop 7"
                     : "+v"(v[0]), "+v"(v[1]), "+v"(v[2]), "+v"(v[3]), "+v"(v[4]), "+v"(v[5]), "+v"(v[6]), "+v"(v[7]), "+v"(ks[i][0]), "+v"(ks[i][1])::"memory");
        float r[8];
#pragma unroll
        for (int j = 0; j < 8; ++j) r[j] = mul1(ks[i][j >> 2][j & 3], v[j]);
        *lds<u32x4>(sa + i * 4096) = (u32x4){pk2(r[7], r[6]), pk2(r[5], r[4]), pk2(r[3], r[2]), pk2(r[1], r[0])};
#pragma unroll
        for (int j = 0; j < 8; ++j) r[j] = mul1(r[j], asf(dd[i]));
        md[i] = (u32x4){pk2(r[0], r[1]), pk2(r[2], r[3]), pk2(r[4], r[5]), pk2(r[6], r[7])};
        SB();
      };
      prod(IC<0>{});
      prod(IC<1>{});
      if (wq4 < 2) prod(IC<2>{});
      calc(0);
      calc(1);
      if (wq4 < 2) calc(2);
      lgkm0();
      lds_arrive(CNT(C_K0) + (wq4 >> 1) * 4);
      lds_wait(CNT(C_K0) + (wq4 >> 1) * 4, 2);
#pragma unroll
      for (int i = 0; i < 3; ++i)
        if (i < 2 || wq4 < 2) __builtin_amdgcn_raw_buffer_store_b128(*lds<u32x4>(ra + i * 4096), rsW, so + i * 16384, 0, SC1);
      lds_arrive(CNT(C_K0) + (wq4 >> 1) * 4);
      lds_wait(CNT(C_K0) + (wq4 >> 1) * 4, 4);
#pragma unroll
      for (int i = 0; i < 3; ++i)
        if (i < 2 || wq4 < 2) *lds<u32x4>(sa + i * 4096) = md[i];
    }
    const u32 img = L_CV + wq4 * CV_CH;
    auto put_h = [&](u32 a, const u32x4 (&ht)[4]) {
#pragma unroll
      for (int i = 0; i < 4; ++i) {
        *lds<u32x4>(a + 32 + i * 1024) = ht[i];
        asm volatile("ds_write2_b32 %0, %1, %2 offset0:7 offset1:8\n\tds_write2_b32 %0, %3, %4 offset0:9 offset1:10"
                     ::"v"(a + 8320 + i * 1024), "v"(ht[i][0]), "v"(ht[i][1]), "v"(ht[i][2]), "v"(ht[i][3]) : "memory");
      }
    };
    asm volatile("" : "+v"(ht[3])::"memory");
    mdma(2);
    mdma(3);
    if (wq4 < 3) {
      if (lane < 40) *lds<u32x4>(img + (lane < 8 ? (lane >> 1) * 4160 + (lane & 1) * 16 : 16640 + 4096 + (lane - 8) * 16)) = (u32x4){0u, 0u, 0u, 0u};
      lgkm0();
      if (lane == 0) {
        *lds<u16>(img + 4160 + 30) = (u16)ht[0][0];
        *lds<u16>(img + 3 * 4160 + 26) = (u16)ht[0][0];
      }
      put_h(img + lane * 16, ht);
      u32 hn[4];
#pragma unroll
      for (int i = 0; i < 4; ++i) hn[i] = *lds<u32>(img + 48 + i * 1024 + lane * 16);
#pragma unroll
      for (int i = 0; i < 4; ++i)
        ht[i] = (u32x4){__builtin_amdgcn_alignbit(ht[i][1], ht[i][0], 16), __builtin_amdgcn_alignbit(ht[i][2], ht[i][1], 16),
                        __builtin_amdgcn_alignbit(ht[i][3], ht[i][2], 16), __builtin_amdgcn_alignbit(hn[i], ht[i][3], 16)};
    }
    vmw<2>();
    if (lane == 0) put_flag(ws + WS_FA1 + ((hq * 4 + wq4) * 32 + g) * FSA, tag);
    if (wq4 < 3) put_h(img + 4160 + lane * 16, ht);
    u32x4(&t)[4] = ht;
#pragma unroll
    for (int st = 1; st < 4; ++st) {
      vm0();
      mdma(2 * st + 2);
      mdma(2 * st + 3);
      mstage(st);
    }
    if (wq4 < 3) wait_flags<8>(ws + WS_FA1 + ((hq * 4 + ((chan(wq4) >> 3) & 2) + h) * 32 + l31) * FSA, true, tag);
    asm volatile("s_waitcnt vmcnt(0) lgkmcnt(0)" ::: "memory");
#pragma unroll
    for (int i = 0; i < 4; ++i)
      t[i] = __builtin_amdgcn_raw_buffer_load_b128(rsW, h * 40960 + l31 * 16, wq4 < 3 ? WS_KVT + hq * HS + (2 * i) * 40960 + chan(wq4) * 512 : OOB, SC1);
    SB();
    mstage(4);
#pragma unroll
    for (int i = 0; i < 3; ++i) {
      const u32 a = L_WM + 16384 + (((wq4 + 1 + i) & 3) * 3 + i) * 2048 + lane * 16;
      *lds<f32x4>(a) = ma[i][0];
      *lds<f32x4>(a + 1024) = ma[i][1];
    }
    SB();
    if (wq4 < 3) {
#pragma unroll
      for (int i = 0; i < 4; ++i) *lds<u32x4>(img + 16640 + lane * 16 + i * 1024) = t[i];
    }
    lds_arrive(CNT(C_WM));
    put_o((u32x4){~0u, ~0u, ~0u, ~0u}, (u32x2){~0u, ~0u});
    tile64(L_QT, Aq);
    lds_wait<1>(CNT(C_WM), 4);
    {
      f32x4 p[6];
#pragma unroll
      for (int i = 0; i < 6; ++i) p[i] = *lds<f32x4>(L_WM + 16384 + wq4 * 6144 + i * 1024 + lane * 16);
      SB();
      const f32x4 a0 = add4(add4(ma[3][0], p[0]), add4(p[2], p[4])), a1 = add4(add4(ma[3][1], p[1]), add4(p[3], p[5]));
      *lds<u32x2>(qa0) = (u32x2){pk2(a0[0], a0[1]), pk2(a0[2], a0[3])};
      *lds<u32x2>(qa1) = (u32x2){pk2(a1[0], a1[1]), pk2(a1[2], a1[3])};
      lds_arrive(CNT(C_CS));
    }
    if (g & 1) conv_shared<0x3B, 6>(L_CV, wq4, lane);
    else conv_shared<0xC4, 8>(L_CV, wq4, lane);
    LOAD_H1(A, rec0);
    LOAD_H2(A, rec0);
#pragma unroll
    for (int i = 0; i < 16; ++i) {
      const f32x4 z = *(volatile LAS f32x4*)(size_t)(L_CV + CV_CH + 16640 + 4096);
#pragma unroll
      for (int j = 0; j < 4; ++j) O[i >> 2][4 * (i & 3) + j] = z[j];
    }
    lds_wait<1>(CNT(C_CS), 4);
    read_q();
    vmw<8>();
    lds_arrive(CNT(C_CH));
    LOAD_H1(B, rec0 + 7168);
    LOAD_H2(B, rec0 + 7168);
    __builtin_amdgcn_s_setprio(1);
  } else {
    f32x4 acc[3];
    u32 qa[3];
#pragma unroll
    for (int i = 0; i < 3; ++i) {
      acc[i] = (f32x4){0.f, 0.f, 0.f, 0.f};
      qa[i] = qaddr(2 * hp + i);
    }
    const u32 wl = L_RING + (16 * (gq >> 1) + 4 * (gq & 1) + ((lane >> 2) & 3)) * 96 + 8 * (lane & 3);
    bf16x8 xf[3][4], wf[2][4][3];
    u64 fl = 0;
    zero_o();
    u32 kp2[2], jo2[2];
#pragma unroll
    for (int i = 0; i < 2; ++i) {
      const u32 c = (i ? 256 : 64 * wq4) + lane, pos = c / 10;
      kp2[i] = swap23(pos);
      jo2[i] = (c - pos * 10) * 16;
    }
    vm0_ = (kp2[0] + (kp2[0] >> 4) * 4) * 1280 + jo2[0]; vm1_ = (kp2[1] + 4) * 1280 + jo2[1];
    auto trow = [&](u32 kp) { return (kp >> 3) * 80 + ((kp >> 2) & 1) * 20 + 16 + (kp & 3); };
    vt0 = (trow(kp2[0]) + (kp2[0] >> 4) * 40) * 1280 + jo2[0]; vt2 = (trow(kp2[0]) + 40 - (kp2[0] >> 4) * 40) * 1280 + jo2[0];
    vt1 = trow(kp2[1]) * 1280 + jo2[1];
    asm volatile("" : "+v"(vm0_), "+v"(vm1_), "+v"(vt0), "+v"(vt1), "+v"(vt2), "+v"(oa0), "+v"(oa1));
#pragma unroll
    for (int s = 0; s < 5; ++s) {
      if (s == 0) vmw<8>();
      if (s == 1) vm0();
      bar();
      if (s == 1) get_tag();
      if (s == 2) fl = __hip_atomic_load((GAS u64*)kvflag, __ATOMIC_RELAXED, __HIP_MEMORY_SCOPE_AGENT);
      const u32 xo = mod5(rot + s + 1) * 16896;
      auto rdw = [&](int ks) {
#pragma unroll
        for (int i = 0; i < 3; ++i) {
          const u32 wb = wl + s * 12288 + ks * 3072 + i * 32;
          wf[s & 1][ks][i] = cat8(trread(wb), trread(wb + 768));
        }
        if (s == 0) xf[0][ks] = *lds<bf16x8>(xa[ks] + rot * 16896);
        if (s < 4) xf[(s + 1) % 3][ks] = *lds<bf16x8>(xa[ks] + xo);
      };
      auto mma = [&](int st, int ks) {
#pragma unroll
        for (int i = 0; i < 3; ++i) acc[i] = __builtin_amdgcn_mfma_f32_16x16x32_bf16(wf[st & 1][ks][i], xf[st % 3][ks], acc[i], 0, 0, 0);
      };
#pragma unroll
      for (int ks = 0; ks < 4; ++ks) {
        rdw(ks);
        SB();
        if (s > 0 && s < 4) mma(s - 1, ks);
        SB();
      }
      if (s == 3) {
        if (!__all(fl == tag)) wait_flags<4>(kvflag, true, tag);
        asm volatile("" ::: "memory");
        LOAD_H1(A, rec0);
        SB();
      }
      if (s == 4) { LOAD_H1(B, rec0 + 7168); SB(); }
      if (s >= 3) {
#pragma unroll
        for (int ks = 0; ks < 4; ++ks) mma(s, ks);
        SB();
      }
      if (s == 3) LOAD_H2(A, rec0);
    }
#pragma unroll
    for (int i = 0; i < 3; ++i) *lds<u32x2>(qa[i]) = (u32x2){pk2(acc[i][0], acc[i][1]), pk2(acc[i][2], acc[i][3])};
    LOAD_H2(B, rec0 + 7168);
    bar();
    read_q();
    __builtin_amdgcn_s_setprio(2);
  }
  u32x4 yv[4];
  auto yload = [&]() {
#pragma unroll
    for (int i = 0; i < 4; ++i)
      yv[i] = __builtin_amdgcn_raw_buffer_load_b128(rsW, (lane >> 3) * 512 + (lane & 7) * 16,
                                                    wq4 * 4 + i < 10 ? WS_YT + hq * HS + ((g >> 2)) * 40960 + (g & 3) * 128 + (wq4 * 4 + i) * 4096 : OOB, SC1);
    SB();
  };
  {
    const int idx = (int)0xDDDDDDDDu;
    const u32 cntA = half ? CNT(C_AT1) : CNT(C_AT0);
    WAIT_H1(A, 12);
    f32x16 Sx[4];
    Sx[0] = qk16(A.k1a, q1[0]); SB();
    Sx[0] = qk8(A.k2a, q2[0], Sx[0]); SB();
    Sx[1] = qk16(A.k1a, q1[1]); SB();
    Sx[1] = qk8(A.k2a, q2[1], Sx[1]); SB();
    __builtin_amdgcn_s_waitcnt(0xC07F);
    SB();
    REC_STEPS(A, B, 0); REC_STEPS(B, A, 1); REC_STEPS(A, B, 2); REC_STEPS(B, A, 3);
    REC_STEPS(A, B, 4); REC_STEPS(B, A, 5); REC_STEPS(A, B, 6);
    const u32 ored = half ? L_OY : L_OX;
    u32 ga;
    auto give = [&](int i, int j) {
      if (j == 0) { ga = ored + (((wq4 + 1 + i) & 3) * 3 + i) * 3072 + lane * 16; asm volatile("" : "+v"(ga)); }
      *lds<f32x4>(ga + j * 1024) = (f32x4){O[i][4 * j], O[i][4 * j + 1], O[i][4 * j + 2], O[i][4 * j + 3]};
    };
    u32 cdv;
    const u32 cfree = half ? CNT(C_CD) : CNT(C_CS);
    STEPX(0, 0, 3, 1, A.v3, B.v0, B.k1a, B.k2a, 1, , cdv = *(volatile LAS u32*)(size_t)cfree);
    STEPX(1, 0, 0, 1, B.v1, B.v2, B.k1b, B.k2b, 1, ,
          if (__builtin_expect(__builtin_amdgcn_readfirstlane(cdv) < 4, 0)) lds_wait<1>(cfree, 4));
    STEPX(2, 1, 0, 2, B.v3, B.v0, B.k1a, B.k2a, 1, , give(0, 0));
    STEPX(3, 1, 1, 2, B.v1, B.v2, B.k1b, B.k2b, 1, give(0, 1), give(0, 2));
    STEPX(0, 2, 1, 3, B.v3, B.v0, B.k1a, B.k2a, 1, , give(1, 0));
    STEPX(1, 2, 2, 3, B.v1, B.v2, B.k1b, B.k2b, 1, give(1, 1), give(1, 2));
    STEPX(2, 3, 2, 0, B.v3, B.v0, B.k1a, B.k2a, 0, , give(2, 0); give(2, 1); give(2, 2));
    EXP4(Sx[3], 0); SB();
    O[3] = pv_hi(B.v1, Sx[2], O[3], idx); SB();
    EXP4(Sx[3], 4); SB();
    EXP4(Sx[3], 8); SB();
    O[3] = pv_lo(B.v2, Sx[3], O[3], idx); SB();
    lds_arrive(cntA);
    EXP4(Sx[3], 12); SB();
    O[3] = pv_hi(B.v3, Sx[3], O[3], idx); SB();
    lds_wait(cntA, 4);
    f32x4 pr[9], t[3];
#pragma unroll
    for (int i = 0; i < 9; ++i) pr[i] = *lds<f32x4>(ored + wq4 * 9216 + lane * 16 + (8 - i) * 1024);
    SB();
    if (half == 1) yload();
#pragma unroll
    for (int i = 0; i < 3; ++i) t[i] = add4(add4((f32x4){O[3][4 * i], O[3][4 * i + 1], O[3][4 * i + 2], O[3][4 * i + 3]}, pr[2 - i]), add4(pr[5 - i], pr[8 - i]));
    float lsum = upper_half(t[2][0]);
    if (__builtin_expect(!__all(lsum >= 5.421010862427522e-20f && lsum <= 1.8446744073709552e19f), 0)) {
      float m = -INFINITY;
#pragma unroll 1
      for (int r = 0; r < 64; ++r) {
        const u32 so = WS_REC + (r >> 1) * 7168;
        const f32x16 sa = qk8(LD64(2048, so + (r & 1) * 512), q2[3], qk16(LD128(0, so + (r & 1) * 1024), q1[3]));
#pragma unroll
        for (int i = 0; i < 16; ++i) m = fmaxf(m, sa[i]);
      }
      const auto mm = __builtin_amdgcn_permlane32_swap(__builtin_bit_cast(u32, m), __builtin_bit_cast(u32, m), false, false);
      const float M = -fmaxf(asf(mm[0]), asf(mm[1]));
      const u32 m0 = __builtin_bit_cast(u32, M) & 0xffff0000u;
      const float r1 = M - __builtin_bit_cast(float, m0);
      const u32 m1 = __builtin_bit_cast(u32, r1) & 0xffff0000u;
      const float r2 = r1 - __builtin_bit_cast(float, m1);
      const u32x2 qs = h ? (u32x2){(m0 >> 16) | m1, __builtin_bit_cast(u32, r2) >> 16} : q2[3];
      f32x16 o;
#pragma unroll
      for (int i = 0; i < 16; ++i) o[i] = 0.f;
#pragma unroll 1
      for (int r = 0; r < 64; ++r) {
        const u32 so = WS_REC + (r >> 1) * 7168;
        f32x16 p = qk8(LD64(2048, so + (r & 1) * 512), qs, qk16(LD128(0, so + (r & 1) * 1024), q1[3]));
        const u32x4 va = LDV(0, so + 3072 + (r & 1) * 2048), vb = LDV(1024, so + 3072 + (r & 1) * 2048);
        EXP4(p, 0); EXP4(p, 4); EXP4(p, 8); EXP4(p, 12);
        o = pv_hi(vb, p, pv_lo(va, p, o, idx), idx);
      }
      asm volatile("s_nop 15" : "+v"(o));
#pragma unroll
      for (int i = 0; i < 3; ++i) t[i] = (f32x4){o[4 * i], o[4 * i + 1], o[4 * i + 2], o[4 * i + 3]};
      lsum = upper_half(o[8]);
    }
    float inv = __builtin_amdgcn_rcpf(lsum);
    asm volatile("s_nop 0" : "+v"(inv));
    auto fin = [&](float a, float b) { return pk2s(mul1(a, inv), mul1(b, inv)); };
    put_o((u32x4){fin(t[0][0], t[0][1]), fin(t[0][2], t[0][3]), fin(t[1][0], t[1][1]), fin(t[1][2], t[1][3])},
          (u32x2){fin(t[2][0], t[2][1]), fin(t[2][2], t[2][3])});
  }
  char* const oflag = ws + WS_FO + ((g * 8 + hq) * 4 + wq4) * FSO;
  const rsrc_t rsOut = mkrs(out, S * 2 * D * 2);
  if (half == 1) {
    __builtin_amdgcn_s_sleep(12);
    lds_arrive(CNT(C_PUB));
    if (wq4 < 3) {
#pragma unroll 1
      for (u32 spins = 0; spins < SPIN_CAP; ++spins) {
        u32 mx = 0;
#pragma unroll
        for (int i = 0; i < 4; ++i) mx = umax_(umax_(umax_(umax_(mx, yv[i][0]), yv[i][1]), yv[i][2]), yv[i][3]);
        if (__all(mx != ~0u)) break;
        lds_wait<1>(CNT(C_A3), 8);
        yload();
      }
      lds_wait<1>(CNT(C_GO), 3);
      const u32 scr = L_GO + wq4 * 8192 + 4096;
      u32x4 qq[4], md[4];
#pragma unroll
      for (int i = 0; i < 4; ++i) {
        qq[i] = *lds<u32x4>(scr - 4096 + i * 1024 + lane * 16);
        md[i] = *lds<u32x4>(scr + (wq4 < 2 ? i : (i & 1)) * 1024 + lane * 16);
      }
      lgkm0();
#pragma unroll
      for (int i = 0; i < 4; ++i) {
        u32x4 y2;
#pragma unroll
        for (int j = 0; j < 4; ++j) y2[j] = pk2(add1(lo2f(yv[i][j]), lo2f(md[i][j])), add1(hi2f(yv[i][j]), hi2f(md[i][j])));
        const u32 chl = 8 * i + (lane >> 3);
        if (wq4 < 2 || i < 2) *lds<u32x4>(scr + chl * 128 + ((lane & 7) ^ (chl >> 2)) * 16) = y2;
      }
      lgkm0();
      const u32 run = lane >> 3;
      u32x4 z[4];
      u32x2 res[8];
#pragma unroll
      for (int c = 0; c < 4; ++c) z[c] = *lds<u32x4>(scr + (((lane & 7) * 4 + c) & (wq4 < 2 ? 31 : 15)) * 128 + (run ^ (lane & 7)) * 16);
#pragma unroll
      for (int i = 0; i < 8; ++i) {
        float r[4];
#pragma unroll
        for (int c = 0; c < 4; ++c) {
          const u32 qv = qq[i >> 1][2 * (i & 1) + (c >> 1)];
          r[c] = mul1((i & 1) ? hi2f(z[c][i >> 1]) : lo2f(z[c][i >> 1]), (c & 1) ? hi2f(qv) : lo2f(qv));
        }
        res[i] = (u32x2){pk2(r[0], r[1]), pk2(r[2], r[3])};
      }
#pragma unroll
      for (int i = 0; i < 8; ++i) *lds<u32x2>(scr - 4096 + (8 * run + i) * 64 + ((lane & 7) ^ (run & 6)) * 8) = res[i];
      lgkm0();
      u32x4 fo[4];
#pragma unroll
      for (int i = 0; i < 4; ++i) fo[i] = *lds<u32x4>(scr - 4096 + i * 1024 + (lane >> 2) * 64 + ((lane & 3) ^ i) * 16);
      const u32 ob = wq4 * 4 + (lane & 3) < 10 ? (64 * g + (lane >> 2)) * 2560 + hq * 160 + wq4 * 64 + (lane & 3) * 16 : OOB;
      lds_wait<1>(CNT(C_SW), 4);
      asm volatile(".Lpf_end:");
#pragma unroll
      for (int i = 0; i < 4; ++i) __builtin_amdgcn_raw_buffer_store_b128(fo[i], rsOut, ob + i * 40960, 0, 0);
    }
    __builtin_amdgcn_endpgm();
  }
  {
    if (blockIdx.x == 0 && threadIdx.x == 0) put_flag(ws + WS_E, tag);
    lds_wait<2>(CNT(C_CH), 4);
    u32x4 kvv[4];
    u32x2 qr[8];
#pragma unroll
    for (int i = 0; i < 8; ++i) qr[i] = *lds<u32x2>(L_QT + (8 * (lane >> 3) + i) * 160 + (w * 8 + (lane & 7)) * 8);
#pragma unroll
    for (int i = 0; i < 4; ++i) {
      const u32 chl = 8 * i + (lane >> 3);
      kvv[i] = *lds<u32x4>(L_A1 + w * 4096 + chl * 128 + ((7 - (lane & 7)) ^ (chl & 7)) * 16);
    }
    SB();
    const rsrc_t rsO = mkrs(Bwo, D * D * 2);
    auto oload = [&](u32x4 (&xf)[10], int kh) {
#pragma unroll
      for (int ks = 0; ks < 10; ++ks)
        xf[ks] = __builtin_amdgcn_raw_buffer_load_b128(rsW, lane * 16 + (ks & 3) * 1024, WS_O + g * GS + (w * 20 + 10 * kh + (ks & ~3)) * 1024, SC1);
      SB();
    };
    u32x4 xe[10];
    {
      const u32 img = L_CV + (1 + (wq4 >> 1)) * CV_CH;
      const u32 xb = conv_xb(img, lane) + (wq4 & 1) * 256, hb = conv_hb(img, lane) + (wq4 & 1) * 256;
      f32x4 acc[8];
#pragma unroll
      for (int i = 0; i < 8; ++i) acc[i] = (f32x4){0.f, 0.f, 0.f, 0.f};
      auto wo_dma = [&](int n) {
        const int q = (n / 5 + g) & 3, e = q >> 1, c = 4 * (q & 1) + (n % 5 < 4 ? n % 5 : wq4);
        if (n < 20) dma16(L_W + (10 * e + c) * 5120 + (n % 5 < 4 ? wq4 : 4) * 1024, n % 5 < 4 ? vm0_ : vm1_, rsO, (80 * c + 40 * (((c >> 1) & 1) ^ e)) * 1280 + hq * 160);
        else if (n < 24) dma16(L_W + (10 * ((n >> 1) & 1) + 8 + (n & 1)) * 5120 + wq4 * 1024, (n & 2) ? vt2 : vt0, rsO, (n & 1) * 320 * 1280 + hq * 160);
        else dma16(L_W + (10 * (wq4 >> 1) + 8 + (wq4 & 1)) * 5120 + 4096, vt1, rsO, ((wq4 & 1) * 320 + 40 * (1 - (wq4 >> 1))) * 1280 + hq * 160);
      };
      auto residue = [&](auto BB, const CFrag<8>& f, CFrag<8>& n) {
        constexpr int B = decltype(BB)::value;
        static_for<36>([&](auto NN) {
          constexpr int N = decltype(NN)::value, a = tri_a(N), c = 7 - a - (N - tri_0(a));
          acc[a + c] = __builtin_amdgcn_mfma_f32_16x16x32_bf16(__builtin_bit_cast(bf16x8, f.a[a]), __builtin_bit_cast(bf16x8, f.b[c]), acc[a + c], 0, 0, 0);
          if constexpr (B < 3 && N < 8) n.b[7 - N] = *lds<u32x4>(xb + 64 * (B + 1) + 512 * N);
          if constexpr (B < 3 && N >= 8 && N < 24) {
            const u32x2 p = *lds<u32x2>(hb + 64 * (B + 1) + 512 * ((N - 8) >> 1) + 8 * (N & 1));
            n.a[(N - 8) >> 1][2 * (N & 1)] = p[0];
            n.a[(N - 8) >> 1][2 * (N & 1) + 1] = p[1];
          }
          if constexpr (B > 0 && N % 4 == 0 && (B - 1) * 9 + N / 4 < 25) wo_dma((B - 1) * 9 + N / 4);
          SB();
        });
      };
      CFrag<8> f0, f1;
      conv_load(f0, hb, xb);
      SB();
      if (wq4 < 3) {
        const u32 n = 2 * wq4 + h;
        const u32 tile = (((g & 1) ? 0x54310u : 0x762u) >> (4 * n)) & 7;
        f32x4 p[8];
#pragma unroll
        for (int i = 0; i < 8; ++i) p[i] = *lds<f32x4>(L_CRED + (i >> 1) * 5120 + (i & 1) * 16 + wq4 * 2048 + lane * 32);
        SB();
        const f32x4 a = add4(add4(p[0], p[2]), add4(p[4], p[6])), b = add4(add4(p[1], p[3]), add4(p[5], p[7]));
        __builtin_amdgcn_raw_buffer_store_b128((u32x4){pk2s(a[0], a[1]), pk2s(a[2], a[3]), pk2s(b[0], b[1]), pk2s(b[2], b[3])}, rsW,
                                               n < ((g & 1) ? 5u : 3u) ? WS_YT + hq * HS + (tile) * 40960 + chan(0) * 512 + l31 * 16 : OOB, 0, SC1);
      }
      lds_arrive(CNT(C_OR));
      __builtin_amdgcn_s_setprio(0);
      residue(IC<0>{}, f0, f1);
      __builtin_amdgcn_s_setprio(2);
      vm0();
      if (lane == 0) put_flag(oflag, tag);
      lds_wait<1>(CNT(C_OR), 4);
      const char* eflag = ws + WS_FO + ((g * 8 + (l15 >> 1)) * 4 + (w & 2) + (l15 & 1)) * FSO;
      asm volatile("" : "+v"(eflag));
      __builtin_amdgcn_s_setprio(0);
      residue(IC<1>{}, f1, f0);
      residue(IC<2>{}, f0, f1);
      __builtin_amdgcn_s_setprio(2);
      residue(IC<3>{}, f1, f0);
      const u64 ef = __hip_atomic_load((GAS u64*)eflag, __ATOMIC_RELAXED, __HIP_MEMORY_SCOPE_AGENT);
      SB();
      const u32 xch = L_CV + 4096;
      const u32 stg = img + (wq4 & 1) * 2048;
      auto swap_tiles = [&](auto KK) {
        constexpr int K = decltype(KK)::value;
#pragma unroll
        for (int i = 0; i < 4; ++i) *lds<f32x4>(xch + wq4 * 4096 + i * 1024 + lane * 16) = acc[4 - K + i];
        lds_arrive(CNT(C_P0) + (wq4 >> 1) * 4);
        lds_wait<1>(CNT(C_P0) + (wq4 >> 1) * 4, 2);
#pragma unroll
        for (int i = 0; i < 4; ++i) {
          const f32x4 v = add4(acc[K + i], *lds<f32x4>(xch + (wq4 ^ 1) * 4096 + i * 1024 + lane * 16));
          *lds<u32x2>(stg + 512 * i + 32 * l15 + 8 * gq) = (u32x2){pk2s(v[0], v[1]), pk2s(v[2], v[3])};
        }
      };
      if (wq4 & 1) swap_tiles(IC<4>{}); else swap_tiles(IC<0>{});
      lgkm0();
      const u32 yo = WS_YT + hq * HS + ((wq4 & 1) * 4 + h) * 40960 + chan(1 + (wq4 >> 1)) * 512 + l31 * 16;
      __builtin_amdgcn_raw_buffer_store_b128(*lds<u32x4>(stg + lane * 16), rsW, yo, 0, SC1);
      __builtin_amdgcn_raw_buffer_store_b128(*lds<u32x4>(stg + 1024 + lane * 16), rsW, yo + 81920, 0, SC1);
      vmw<2>();
      lds_arrive(CNT(C_CD));
      if (!__all(ef == tag)) wait_flags<2>(eflag, true, tag);
      asm volatile("" ::: "memory");
      oload(xe, 0);
    }
    lds_wait<1>(CNT(C_CD), 4);
    if (w < 3) {
#pragma unroll
      for (int i = 0; i < 4; ++i) {
        *lds<u32x4>(L_GO + w * 8192 + i * 1024 + lane * 16) = (u32x4){qr[2 * i][0], qr[2 * i][1], qr[2 * i + 1][0], qr[2 * i + 1][1]};
        if (w < 2 || i < 2) *lds<u32x4>(L_GO + w * 8192 + 4096 + i * 1024 + lane * 16) = kvv[i];
      }
      lds_arrive(CNT(C_GO));
    }
    f32x4 acc[5];
#pragma unroll
    for (int i = 0; i < 5; ++i) acc[i] = (f32x4){0.f, 0.f, 0.f, 0.f};
    const u32 wl = L_W + (16 * (gq >> 1) + 4 * (gq & 1) + ((lane >> 2) & 3)) * 160 + 8 * (lane & 3);
    auto wfrag = [&](u32 wb, int ks, int nt) { return cat8(trread(wb + ks * 5120 + nt * 32), trread(wb + ks * 5120 + 1280 + nt * 32)); };
    {
      lds_wait<2>(CNT(C_AT1), 4);
      __builtin_amdgcn_s_setprio(0);
      const u32 wb = wl;
      bf16x8 wf[2][5];
#pragma unroll
      for (int nt = 0; nt < 5; ++nt) wf[0][nt] = wfrag(wb, 0, nt);
      SB();
#pragma unroll
      for (int ks = 0; ks < 10; ++ks) {
#pragma unroll
        for (int nt = 0; nt < 5; ++nt) {
          if (ks < 9) wf[(ks + 1) & 1][nt] = wfrag(wb, ks + 1, nt);
          acc[nt] = __builtin_amdgcn_mfma_f32_16x16x32_bf16(wf[ks & 1][nt], __builtin_bit_cast(bf16x8, xe[ks]), acc[nt], 0, 0, 0);
        }
        SB();
      }
    }
    SB();
    {
      const int kh = 1;
      u32 wb = wl + kh * 51200;
      asm volatile("" : "+v"(wb));
      constexpr int NPRE = 4;
      bf16x8 wf[NPRE][5];
#pragma unroll
      for (int ks = 0; ks < NPRE; ++ks)
#pragma unroll
        for (int nt = 0; nt < 5; ++nt) wf[ks][nt] = wfrag(wb, ks, nt);
      SB();
      lds_arrive(CNT(C_A3));
      __builtin_amdgcn_s_setprio(2);
      lds_wait<1>(CNT(C_A3), 4);
      lds_wait<1>(CNT(C_PUB), 4);
      f32x4 res[5];
#pragma unroll 1
      for (u32 spins = 0; spins < SPIN_CAP; ++spins) {
        u32x4 xf[10];
        asm volatile("" ::: "memory");
        oload(xf, kh);
        lds_arrive(CNT(C_SW));
        bf16x8 wl2[2][5];
#pragma unroll
        for (int ks = 0; ks < 2; ++ks)
#pragma unroll
          for (int nt = 0; nt < 5; ++nt) wl2[ks][nt] = wfrag(wb, NPRE + ks, nt);
        SB();
        u32 mx = 0;
#pragma unroll
        for (int ks = 0; ks < 10; ++ks) {
#pragma unroll
          for (int nt = 0; nt < 5; ++nt) {
            res[nt] = __builtin_amdgcn_mfma_f32_16x16x32_bf16(ks < NPRE ? wf[ks < NPRE ? ks : 0][nt] : wl2[ks & 1][nt], __builtin_bit_cast(bf16x8, xf[ks]),
                                                              ks ? res[nt] : acc[nt], 0, 0, 0);
            if (ks >= NPRE && ks < 8) wl2[ks & 1][nt] = wfrag(wb, ks + 2, nt);
          }
          mx = umax_(umax_(umax_(umax_(mx, xf[ks][0]), xf[ks][1]), xf[ks][2]), xf[ks][3]);
          SB();
        }
        if (__all(mx != ~0u)) break;
      }
      lds_arrive(CNT(C_A3));
#pragma unroll
      for (int nt = 0; nt < 5; ++nt) acc[nt] = res[nt];
    }
    const u32 stg = L_W + w * 4096;
#pragma unroll
    for (int i = 0; i < 5; ++i) *lds<u32x2>(stg + l15 * 160 + i * 32 + gq * 8) = (u32x2){pk2(acc[i][0], acc[i][1]), pk2(acc[i][2], acc[i][3])};
    lgkm0();
    const u32 ob = (64 * g + 16 * w) * 2560 + 1280 + hq * 160;
#pragma unroll
    for (int i = 0; i < 3; ++i) {
      const u32 c = i < 2 ? i * 64 + lane : 128 + l31, r = c / 10;
      __builtin_amdgcn_raw_buffer_store_b128(*lds<u32x4>(stg + c * 16), rsOut, (i == 2 && h) ? OOB : ob + r * 2560 + (c - r * 10) * 16, 0, 0);
    }
  }
}

extern "C" void kernel_launch(void* const* d_in, const int* in_sizes, int n_in, void* d_out, int out_size, void* d_ws, size_t ws_size,
                              hipStream_t stream) {
  fused_kernel<<<256, 512, 0, stream>>>((const u16*)d_in[6], (const u16*)d_in[8], (const u16*)d_in[9], (const u16*)d_in[7], (char*)d_ws,
                                        (const u16*)d_in[1], (const u16*)d_in[2], (const u16*)d_in[3], (const u16*)d_in[4],
                                        (const u16*)d_in[0], (const u16*)d_in[5], (const u16*)d_in[10], (u16*)d_out);
}
```
